```python
import torch

def _softmax(x, axis=-1):
    return torch.softmax(x, dim=axis)
def _gelu(x):
    return torch.nn.functional.gelu(x, approximate="tanh")
def _silu(x):
    return torch.nn.functional.silu(x)
def _sigmoid(x):
    return torch.sigmoid(x)
def _layernorm(x, axis=-1, eps=1e-5):
    m = x.mean(dim=axis, keepdim=True)
    v = x.var(dim=axis, keepdim=True, unbiased=False)
    return (x - m) / torch.sqrt(v + eps)
def _scatter_add(tgt, idx, src, axis=0):
    return tgt.clone().index_add_(axis, idx, src)
def _segment_sum(src, seg, n):
    out = torch.zeros((n,) + src.shape[1:], dtype=src.dtype)
    return out.index_add_(0, seg, src)
def _pad(x, pw):
    flat = tuple(v for pair in reversed(pw) for v in pair)
    return torch.nn.functional.pad(x, flat)

kernel_name = 'interleave_layers__gqa_block_cached__dense_activation_gelu__decode'

def setup_inputs():

    _w = (lambda a: (lambda t: t.to(torch.bfloat16) if t.is_floating_point() else t)(torch.as_tensor(a)))
    def _A_setup():
        B, Sq, D = 2, 1, 512
        Hq, Hkv, Sk, Dh = 32, 32, 4096, 128
        assert Hq % Hkv == 0, (Hq, Hkv)
        return {
            "x": _w((lambda *s: torch.randn(*s, dtype=torch.bfloat16) * 0.05)(B, Sq, D)),
            "k_cache": _w((lambda *s: torch.randn(*s, dtype=torch.bfloat16) * 0.8)(B, Hkv, Sk, Dh)),
            "v_cache": _w((lambda *s: torch.randn(*s, dtype=torch.bfloat16) * 1.6)(B, Hkv, Sk, Dh)),
            "wq": _w((lambda *s: torch.randn(*s, dtype=torch.bfloat16) * 0.8)(D, Hq * Dh) * (D ** -0.5)),
            "wo": _w((lambda *s: torch.randn(*s, dtype=torch.bfloat16) * 1.6)(Hq * Dh, D) * ((Hq * Dh) ** -0.5)),
        }
    _A_in = _A_setup()

    def _B_setup():
        B, S, D, F = 2, 1, 512, 1376
        return {
            "x": _w((lambda *s: torch.randn(*s, dtype=torch.bfloat16) * 0.05)(B, S, D)),
            "w1": _w((lambda *s: torch.randn(*s, dtype=torch.bfloat16) * 0.4)(D, F) * (D ** -0.5)),
            "b1": _w((lambda *s: torch.randn(*s, dtype=torch.bfloat16) * 0.4)(F) * 0.01),
            "w2": _w((lambda *s: torch.randn(*s, dtype=torch.bfloat16) * 0.4)(F, D) * (F ** -0.5)),
            "b2": _w((lambda *s: torch.randn(*s, dtype=torch.bfloat16) * 0.4)(D) * 0.01),
        }
    _B_in = _B_setup()

    return {
        "A_x": _A_in["x"],
        "A_k_cache": _A_in["k_cache"],
        "A_v_cache": _A_in["v_cache"],
        "A_wq": _A_in["wq"],
        "A_wo": _A_in["wo"],
        "B_x": _B_in["x"],
        "B_w1": _B_in["w1"],
        "B_b1": _B_in["b1"],
        "B_w2": _B_in["w2"],
        "B_b2": _B_in["b2"],
    }

def reference(A_x, A_k_cache, A_v_cache, A_wq, A_wo, B_x, B_w1, B_b1, B_w2, B_b2):
    def _A_ref(x, k_cache, v_cache, wq, wo):
        B, Sq, D = x.shape
        Hq, Dh = 32, 128
        Hkv = k_cache.shape[1]
        rep = Hq // Hkv
        q = (lambda x, *d: x.permute(*d))((x @ wq).reshape(B, Sq, Hq, Dh), 0, 2, 1, 3)
        k = (lambda x, n, axis: torch.repeat_interleave(x, n, dim=axis))(k_cache, rep, axis=1)
        v = (lambda x, n, axis: torch.repeat_interleave(x, n, dim=axis))(v_cache, rep, axis=1)
        scores = torch.einsum("bhqd,bhkd->bhqk", q, k) * (Dh ** -0.5)
        p = _softmax(scores, axis=-1)
        o = torch.einsum("bhqk,bhkd->bhqd", p, v)
        return (lambda x, *d: x.permute(*d))(o, 0, 2, 1, 3).reshape(B, Sq, Hq * Dh) @ wo

    def _B_ref(x, w1, b1, w2, b2):
        h = torch.einsum("bsd,df->bsf", x, w1) + b1
        h = _gelu(h)
        return torch.einsum("bsf,fd->bsd", h, w2) + b2

    x = A_x
    x = x + _A_ref(_layernorm(x, axis=-1), A_k_cache, A_v_cache, A_wq, A_wo)
    x = x + _B_ref(_layernorm(x, axis=-1), B_w1, B_b1, B_w2, B_b2)
    x = x + _A_ref(_layernorm(x, axis=-1), A_k_cache, A_v_cache, A_wq, A_wo)
    x = x + _B_ref(_layernorm(x, axis=-1), B_w1, B_b1, B_w2, B_b2)
    return x
```

```cpp
#include <hip/hip_runtime.h>
#include <hip/hip_bf16.h>
#include <stdint.h>

typedef unsigned short u16;
typedef unsigned u32x4 __attribute__((ext_vector_type(4)));
typedef __bf16 bf16x2 __attribute__((ext_vector_type(2)));

constexpr int B = 2, D = 512, H = 32, SK = 4096, DH = 128, F = 1376, NS = 8;

__device__ __forceinline__ float bf2f(u16 x) { return __uint_as_float((unsigned)x << 16); }
__device__ __forceinline__ float rbf(float x) { return (float)(__bf16)x; }
__device__ __forceinline__ u16 f2bf(float x) { __bf16 b = (__bf16)x; return __builtin_bit_cast(u16, b); }
__device__ __forceinline__ float wsum(float v) { for (int o = 32; o; o >>= 1) v += __shfl_xor(v, o); return v; }
__device__ __forceinline__ float wmax(float v) { for (int o = 32; o; o >>= 1) v = fmaxf(v, __shfl_xor(v, o)); return v; }

__device__ void ln_to_lds(const float* x, float* xn) {
  int t = threadIdx.x, w = t >> 6, l = t & 63;
  if (w < B) {
    float v[8], s = 0;
    for (int i = 0; i < 8; i++) { v[i] = x[w * D + l + 64 * i]; s += v[i]; }
    float m = wsum(s) / D, q = 0;
    for (int i = 0; i < 8; i++) { float d = v[i] - m; q += d * d; }
    float r = rsqrtf(wsum(q) / D + 1e-5f);
    for (int i = 0; i < 8; i++) xn[w * D + l + 64 * i] = rbf((v[i] - m) * r);
  }
  __syncthreads();
}

__global__ void k_init(const u16* x, float* xf) { int i = blockIdx.x * 256 + threadIdx.x; xf[i] = bf2f(x[i]); }

template <int N, bool GELU>
__global__ void __launch_bounds__(256) k_ln_gemv(const float* x, const u16* w, const u16* bias, float* out) {
  __shared__ float xn[B * D];
  __shared__ float red[4][B][64];
  ln_to_lds(x, xn);
  int t = threadIdx.x, wv = t >> 6, l = t & 63, n = blockIdx.x * 64 + l;
  float a0 = 0, a1 = 0;
  if (n < N)
    for (int k = wv * 128; k < wv * 128 + 128; k++) { float ww = bf2f(w[(size_t)k * N + n]); a0 += xn[k] * ww; a1 += xn[D + k] * ww; }
  red[wv][0][l] = a0; red[wv][1][l] = a1;
  __syncthreads();
  if (wv < B && n < N) {
    float v = rbf(red[0][wv][l] + red[1][wv][l] + red[2][wv][l] + red[3][wv][l]);
    if (GELU) {
      v = rbf(v + bf2f(bias[n]));
      float u = 0.7978845608028654f * (v + 0.044715f * v * v * v);
      v = rbf(0.5f * v * (1.f + tanhf(u)));
    }
    out[wv * N + n] = v;
  }
}

__global__ void __launch_bounds__(256) k_attn(const float* q, const u16* K, const u16* V, float* po, float* pml) {
  __shared__ float mrg[4][4][DH + 2];
  int s = blockIdx.x, h = blockIdx.y, b = blockIdx.z;
  int t = threadIdx.x, w = t >> 6, l = t & 63, r = l >> 4, c = l & 15;
  constexpr int KPS = SK / NS, KPW = KPS / 4;
  float qv[8];
  for (int i = 0; i < 8; i++) qv[i] = q[b * H * DH + h * DH + 8 * c + i];
  size_t base = ((size_t)(b * H + h) * SK + s * KPS + w * KPW + r) * DH + 8 * c;
  float m = -INFINITY, ls = 0, acc[8] = {0, 0, 0, 0, 0, 0, 0, 0};
  for (int i = 0; i < KPW / 4; i += 8) {
    u32x4 kk[8], vv[8];
#pragma unroll
    for (int j = 0; j < 8; j++) { kk[j] = __builtin_nontemporal_load((const u32x4*)(K + base + (size_t)(i + j) * 4 * DH)); vv[j] = __builtin_nontemporal_load((const u32x4*)(V + base + (size_t)(i + j) * 4 * DH)); }
    float sc[8], mx = m;
#pragma unroll
    for (int j = 0; j < 8; j++) {
      float d = 0;
#pragma unroll
      for (int e = 0; e < 4; e++) { d += __uint_as_float(kk[j][e] << 16) * qv[2 * e]; d += __uint_as_float(kk[j][e] & 0xffff0000u) * qv[2 * e + 1]; }
      d += __shfl_xor(d, 1); d += __shfl_xor(d, 2); d += __shfl_xor(d, 4); d += __shfl_xor(d, 8);
      sc[j] = d * 0.08838834764831845f; mx = fmaxf(mx, sc[j]);
    }
    float al = __expf(m - mx); m = mx; ls *= al;
#pragma unroll
    for (int e = 0; e < 8; e++) acc[e] *= al;
#pragma unroll
    for (int j = 0; j < 8; j++) {
      float p = __expf(sc[j] - m); ls += p;
#pragma unroll
      for (int e = 0; e < 4; e++) { acc[2 * e] += p * __uint_as_float(vv[j][e] << 16); acc[2 * e + 1] += p * __uint_as_float(vv[j][e] & 0xffff0000u); }
    }
  }
  for (int e = 0; e < 8; e++) mrg[w][r][8 * c + e] = acc[e];
  if (c == 0) { mrg[w][r][DH] = m; mrg[w][r][DH + 1] = ls; }
  __syncthreads();
  if (t < DH) {
    float M = -INFINITY;
    for (int i = 0; i < 16; i++) M = fmaxf(M, mrg[i >> 2][i & 3][DH]);
    float L = 0, o = 0;
    for (int i = 0; i < 16; i++) { float wt = __expf(mrg[i >> 2][i & 3][DH] - M); L += wt * mrg[i >> 2][i & 3][DH + 1]; o += wt * mrg[i >> 2][i & 3][t]; }
    int slot = (b * H + h) * NS + s;
    po[slot * DH + t] = o;
    if (t == 0) { pml[2 * slot] = M; pml[2 * slot + 1] = L; }
  }
}

__global__ void __launch_bounds__(256) k_wo(const float* po, const float* pml, const u16* wo, float* py) {
  __shared__ float o[B][DH];
  int h = blockIdx.x, half = blockIdx.y, t = threadIdx.x;
  { int b = t >> 7, d = t & 127, slot = (b * H + h) * NS;
    float M = -INFINITY;
    for (int s = 0; s < NS; s++) M = fmaxf(M, pml[2 * (slot + s)]);
    float L = 0, a = 0;
    for (int s = 0; s < NS; s++) { float wt = __expf(pml[2 * (slot + s)] - M); L += wt * pml[2 * (slot + s) + 1]; a += wt * po[(slot + s) * DH + d]; }
    o[b][d] = rbf(a / L); }
  __syncthreads();
  int n = half * 256 + t;
  float a0 = 0, a1 = 0;
#pragma unroll 16
  for (int k = 0; k < DH; k++) { float ww = bf2f(wo[(size_t)(h * DH + k) * D + n]); a0 += o[0][k] * ww; a1 += o[1][k] * ww; }
  py[(h * B + 0) * D + n] = a0; py[(h * B + 1) * D + n] = a1;
}

__global__ void __launch_bounds__(256) k_w2(const float* hh, const u16* w2, float* py) {
  int g = blockIdx.x, half = blockIdx.y, t = threadIdx.x, n = half * 256 + t;
  float a0 = 0, a1 = 0;
#pragma unroll 16
  for (int k = g * 32; k < g * 32 + 32; k++) { float ww = bf2f(w2[(size_t)k * D + n]); a0 += hh[k] * ww; a1 += hh[F + k] * ww; }
  py[(g * B + 0) * D + n] = a0; py[(g * B + 1) * D + n] = a1;
}

__global__ void __launch_bounds__(256) k_red(const float* py, int np, const float* xin, const u16* bias, float* xout, u16* out) {
  int i = blockIdx.x * 256 + threadIdx.x;
  float a = 0;
  for (int p = 0; p < np; p++) a += py[p * B * D + i];
  a = rbf(a);
  if (bias) a = rbf(a + bf2f(bias[i & (D - 1)]));
  a = rbf(xin[i] + a);
  xout[i] = a;
  if (out) out[i] = f2bf(a);
}

extern "C" void kernel_launch(void* const* d_in, const int* in_sizes, int n_in, void* d_out, int out_size, void* d_ws, size_t ws_size, hipStream_t stream) {
  const u16* x = (const u16*)d_in[0]; const u16* K = (const u16*)d_in[1]; const u16* V = (const u16*)d_in[2];
  const u16* wq = (const u16*)d_in[3]; const u16* wo = (const u16*)d_in[4];
  const u16* w1 = (const u16*)d_in[6]; const u16* b1 = (const u16*)d_in[7]; const u16* w2 = (const u16*)d_in[8]; const u16* b2 = (const u16*)d_in[9];
  float* ws = (float*)d_ws;
  float* xa = ws; float* xb = ws + 1024; float* q = ws + 2048; float* hh = q + 8192; float* pml = hh + 4096; float* po = pml + 2048; float* py = po + B * H * NS * DH;
  k_init<<<4, 256, 0, stream>>>(x, xa);
  for (int L = 0; L < 2; L++) {
    k_ln_gemv<H * DH, false><<<64, 256, 0, stream>>>(xa, wq, nullptr, q);
    k_attn<<<dim3(NS, H, B), 256, 0, stream>>>(q, K, V, po, pml);
    k_wo<<<dim3(H, 2), 256, 0, stream>>>(po, pml, wo, py);
    k_red<<<4, 256, 0, stream>>>(py, H, xa, nullptr, xb, nullptr);
    k_ln_gemv<F, true><<<(F + 63) / 64, 256, 0, stream>>>(xb, w1, b1, hh);
    k_w2<<<dim3(F / 32, 2), 256, 0, stream>>>(hh, w2, py);
    k_red<<<4, 256, 0, stream>>>(py, F / 32, xb, b2, xa, L == 1 ? (u16*)d_out : nullptr);
  }
}
```

```cpp
#include <hip/hip_runtime.h>
#include <stdint.h>

__global__ void __launch_bounds__(256, 1)
decode_layers_kernel(const void* x, const void* k, const void* v, const void* wq, const void* wo, const void* w1,
                     const void* b1, const void* w2, const void* b2, void* out, void* ws) {
  __shared__ __attribute__((aligned(16))) char lds[163840];
  asm volatile(
      "s_memrealtime s[30:31]\n"
      "s_load_dwordx16 s[8:23], %0, 0\n"
      "s_load_dwordx4 s[24:27], %0, 64\n"
      "s_load_dwordx2 s[28:29], %0, 80\n"
      "s_mov_b32 s76, %1\n"
      "s_getpc_b64 s[98:99]\n"
      "L_pc:\n"
      "s_and_b32 s93, s76, 0xf8\n"
      "v_lshrrev_b32 v1, 5, v0\n"
      "v_add_u32 v1, s93, v1\n"
      "v_lshlrev_b32 v1, 7, v1\n"
      "v_min_u32 v1, L_last-L_pc, v1\n"
      "global_load_dword v255, v1, s[98:99]\n"
      "v_lshrrev_b32 v1, 6, v0\n"
      "v_and_b32 v0, 63, v0\n"
      "v_readfirstlane_b32 s77, v1\n"
      "s_and_b32 s78, s76, 7\n"
      "s_lshr_b32 s79, s76, 3\n"
      "s_lshr_b32 s93, s79, 3\n"
      "s_lshl_b32 s94, s78, 2\n"
      "s_add_u32 s93, s93, s94\n"
      "s_bfe_u32 s94, s79, 0x10002\n"
      "s_lshl_b32 s94, s94, 5\n"
      "s_add_u32 s93, s93, s94\n"
      "s_lshl_b32 s93, s93, 2\n"
      "s_and_b32 s94, s79, 3\n"
      "s_add_u32 s93, s93, s94\n"
      "s_lshl_b32 s86, s93, 18\n"
      "s_mul_i32 s92, s77, 34816\n"
      "s_bitcmp1_b32 s77, 0\n"
      "s_cselect_b32 s93, 0x4000, 0\n"
      "s_xor_b32 s86, s86, s93\n"
      "s_bitcmp1_b32 s77, 1\n"
      "s_cselect_b32 s93, 0x800, 0\n"
      "s_xor_b32 s86, s86, s93\n"
      "s_bitcmp1_b32 s76, 3\n"
      "s_cselect_b32 s93, 0x36800, 0\n"
      "s_xor_b32 s86, s86, s93\n"
      "s_bitcmp1_b32 s76, 5\n"
      "s_cselect_b32 s93, 0x8000, 0\n"
      "s_xor_b32 s86, s86, s93\n"
      "s_bitcmp1_b32 s76, 6\n"
      "s_cselect_b32 s93, 0x800, 0\n"
      "s_xor_b32 s86, s86, s93\n"
      "s_bitcmp1_b32 s76, 7\n"
      "s_cselect_b32 s93, 0x16000, 0\n"
      "s_xor_b32 s86, s86, s93\n"
      "s_bitcmp1_b32 s76, 0\n"
      "s_cselect_b32 s93, 0x4000, 0\n"
      "s_xor_b32 s86, s86, s93\n"
      "s_bitcmp1_b32 s76, 2\n"
      "s_cselect_b32 s93, 0xc00, 0\n"
      "s_xor_b32 s86, s86, s93\n"
      "s_and_b32 s87, s86, 0x300\n"
      "s_xor_b32 s86, s86, s87\n"
      "v_and_b32 v7, 15, v0\n"
      "v_lshrrev_b32 v8, 4, v0\n"
      "v_and_b32 v9, 1, v8\n"
      "v_lshlrev_b32 v9, 8, v9\n"
      "v_lshrrev_b32 v10, 1, v8\n"
      "v_lshl_or_b32 v9, v10, 9, v9\n"
      "v_xor_b32 v51, s87, v9\n"
      "v_xor_b32 v50, v7, v8\n"
      "v_lshl_or_b32 v50, v50, 4, v51\n"
      "s_mov_b32 s34, 67108864\n"
      "s_mov_b32 s35, 0x20000\n"
      "s_waitcnt lgkmcnt(0)\n"
      "s_mov_b32 s32, s10\n"
      "s_and_b32 s33, s11, 0xffff\n"
      "s_mov_b32 s70, s30\n"
      "s_add_u32 m0, s92, 17408\n"
      "s_xor_b32 s85, s86, 0\n"
      "buffer_load_dwordx4 v50, s[32:35], s85 offen nt lds\n"
      "s_add_u32 m0, s92, 18496\n"
      "s_xor_b32 s85, s86, 8192\n"
      "buffer_load_dwordx4 v50, s[32:35], s85 offen nt lds\n"
      "s_mov_b32 s56, s18\n"
      "s_and_b32 s57, s19, 0xffff\n"
      "s_mov_b32 s58, 1409024\n"
      "s_mov_b32 s59, 0x20000\n"
      "s_lshl_b32 s97, s79, 2\n"
      "s_add_u32 s97, s97, s77\n"
      "s_mul_i32 s95, s78, 352\n"
      "s_mul_i32 s97, s97, 11008\n"
      "s_add_u32 s95, s95, s97\n"
      "v_lshrrev_b32 v9, 2, v7\n"
      "v_mul_u32_u24 v9, 2752, v9\n"
      "v_and_b32 v10, 3, v7\n"
      "v_lshlrev_b32 v10, 7, v10\n"
      "v_min_u32 v10, 348, v10\n"
      "v_add3_u32 v9, v9, v10, s95\n"
      "v_lshl_or_b32 v9, v8, 24, v9\n"
      "buffer_load_dword v254, v9, s[56:59], 0 offen\n"
      "s_add_u32 m0, s92, 19584\n"
      "s_xor_b32 s85, s86, 65536\n"
      "buffer_load_dwordx4 v50, s[32:35], s85 offen nt lds\n"
      "s_add_u32 m0, s92, 20672\n"
      "s_xor_b32 s85, s86, 73728\n"
      "buffer_load_dwordx4 v50, s[32:35], s85 offen nt lds\n"
      "s_add_u32 m0, s92, 21760\n"
      "s_xor_b32 s85, s86, 4096\n"
      "buffer_load_dwordx4 v50, s[32:35], s85 offen nt lds\n"
      "s_add_u32 m0, s92, 22848\n"
      "s_xor_b32 s85, s86, 12288\n"
      "buffer_load_dwordx4 v50, s[32:35], s85 offen nt lds\n"
      "s_cmp_lg_u32 s77, 3\n"
      "s_cbranch_scc1 L_et_d\n"
      "s_call_b64 s[66:67], L_stub_d\n"
      "L_et_d:\n"
      "s_cmp_lg_u32 s77, 2\n"
      "s_cbranch_scc1 L_et_k93\n"
      "s_call_b64 s[66:67], L_stub_k93\n"
      "L_et_k93:\n"
      "s_cmp_lg_u32 s77, 1\n"
      "s_cbranch_scc1 L_et_k66\n"
      "s_call_b64 s[66:67], L_stub_k66\n"
      "L_et_k66:\n"
      "s_cmp_lg_u32 s77, 0\n"
      "s_cbranch_scc1 L_et_k26\n"
      "s_call_b64 s[66:67], L_stub_k26\n"
      "L_et_k26:\n"
      "s_mov_b32 s80, 0\n"
      "s_mov_b32 s81, 0x100000\n"
      "s_mov_b32 s5, 1\n"
      "s_mul_i32 s88, s78, 11\n"
      "s_lshr_b32 s93, s79, 1\n"
      "s_add_u32 s88, s88, s93\n"
      "s_cmp_lt_u32 s79, 22\n"
      "s_cselect_b32 s91, 1, 0\n"
      "s_cmp_lt_u32 s88, 86\n"
      "s_cselect_b32 s91, s91, 0\n"
      "s_lshr_b32 s93, s79, 4\n"
      "s_lshl_b32 s89, s78, 1\n"
      "s_add_u32 s89, s89, s93\n"
      "s_and_b32 s90, s79, 15\n"
      "v_lshrrev_b32 v9, 3, v0\n"
      "v_lshlrev_b32 v9, 13, v9\n"
      "v_and_b32 v10, 7, v0\n"
      "v_lshl_add_u32 v9, v10, 4, v9\n"
      "s_mov_b32 s48, s14\n"
      "s_and_b32 s49, s15, 0xffff\n"
      "s_mov_b32 s50, 4194304\n"
      "s_mov_b32 s51, 0x20000\n"
      "s_lshl_b32 s94, s89, 18\n"
      "s_lshl_b32 s93, s90, 9\n"
      "s_add_u32 s94, s94, s93\n"
      "s_lshl_b32 s93, s77, 7\n"
      "s_add_u32 s94, s94, s93\n"
      "v_add_u32 v44, s94, v9\n"
      "s_add_u32 m0, s92, 0\n"
      "s_mov_b32 s85, 0\n"
      "buffer_load_dwordx4 v44, s[48:51], s85 offen   lds\n"
      "s_add_u32 m0, s92, 1088\n"
      "s_mov_b32 s85, 65536\n"
      "buffer_load_dwordx4 v44, s[48:51], s85 offen   lds\n"
      "s_add_u32 m0, s92, 2176\n"
      "s_mov_b32 s85, 131072\n"
      "buffer_load_dwordx4 v44, s[48:51], s85 offen   lds\n"
      "s_add_u32 m0, s92, 3264\n"
      "s_mov_b32 s85, 196608\n"
      "buffer_load_dwordx4 v44, s[48:51], s85 offen   lds\n"
      "s_mov_b32 s64, s8\n"
      "s_and_b32 s65, s9, 0xffff\n"
      "s_mov_b32 s66, 2048\n"
      "s_mov_b32 s67, 0x20000\n"
      "s_mov_b32 s72, s24\n"
      "s_and_b32 s73, s25, 0xffff\n"
      "s_mov_b32 s74, 1024\n"
      "s_mov_b32 s75, 0x20000\n"
      "s_mov_b32 s44, s20\n"
      "s_and_b32 s45, s21, 0xffff\n"
      "s_mov_b32 s46, 2752\n"
      "s_mov_b32 s47, 0x20000\n"
      "v_and_b32 v14, 3, v0\n"
      "v_lshlrev_b32 v14, 2, v14\n"
      "v_lshl_add_u32 v14, v8, 4, v14\n"
      "v_and_b32 v15, 4, v0\n"
      "v_lshl_add_u32 v14, v15, 8, v14\n"
      "v_lshlrev_b32 v15, 1, v10\n"
      "v_lshl_add_u32 v15, v8, 4, v15\n"
      "s_lshl_b32 s93, s88, 5\n"
      "v_lshl_add_u32 v13, v7, 1, s93\n"
      "s_lshl_b32 s93, s89, 6\n"
      "buffer_load_dword v32, v14, s[64:67], s93 offen\n"
      "buffer_load_ushort v33, v15, s[72:75], s93 offen\n"
      "buffer_load_ushort v34, v13, s[44:47], 0 offen\n"
      "s_mov_b32 s40, s28\n"
      "s_and_b32 s41, s29, 0xffff\n"
      "s_mov_b32 s42, 2097152\n"
      "s_mov_b32 s43, 0x20000\n"
      "v_mov_b64 v[20:21], -1\n"
      "v_mov_b64 v[22:23], -1\n"
      "v_mov_b32 v192, 0\n"
      "v_mov_b32 v208, 0\n"
      "v_lshlrev_b32 v57, 25, v0\n"
      "v_lshl_or_b32 v57, v1, 23, v57\n"
      "v_lshlrev_b32 v58, 5, v0\n"
      "s_lshl_b32 s7, s76, 2\n"
      "s_add_u32 s7, s7, s77\n"
      "s_cmp_eq_u32 s7, 993\n"
      "s_cselect_b32 s6, 1, 0\n"
      "s_cmp_lt_u32 s7, 1024\n"
      "s_cselect_b32 s94, 0, 0x80000000\n"
      "s_lshl_b32 s93, s7, 8\n"
      "v_lshlrev_b32 v9, 4, v0\n"
      "v_lshl_or_b32 v9, v8, 24, v9\n"
      "v_or_b32 v9, s94, v9\n"
      "buffer_store_dwordx4 v[20:23], v9, s[40:43], s93 offen sc1\n"
      "s_lshl_b32 s7, s76, 5\n"
      "s_add_u32 s7, s7, 1720320\n"
      "buffer_store_dword v208, v57, s[40:43], s7 offen offset:4 sc1\n"
      "s_and_b32 s93, s76, 63\n"
      "s_lshl_b32 s93, s93, 7\n"
      "s_lshr_b32 s94, s76, 6\n"
      "s_lshl_b32 s94, s94, 14\n"
      "s_add_u32 s93, s93, s94\n"
      "s_add_u32 s93, s93, 1736704\n"
      "s_add_u32 s68, s28, s93\n"
      "s_addc_u32 s69, s29, 0\n"
      "v_and_b32 v9, 8, v0\n"
      "v_lshlrev_b32 v9, 10, v9\n"
      "v_lshl_add_u32 v9, v10, 4, v9\n"
      "v_lshl_or_b32 v9, v8, 24, v9\n"
      "v_lshl_or_b32 v9, v1, 26, v9\n"
      "buffer_store_dwordx4 v[20:23], v9, s[40:43], s93 offen sc1\n"
      "s_cmp_eq_u32 s6, 0\n"
      "s_cbranch_scc1 L_noreset\n"
      "s_mov_b32 s93, 1720320\n"
      "buffer_store_dword v208, v58, s[40:43], s93 offen offset:0 sc1\n"
      "s_mov_b32 s93, 1722368\n"
      "buffer_store_dword v208, v58, s[40:43], s93 offen offset:0 sc1\n"
      "s_mov_b32 s93, 1724416\n"
      "buffer_store_dword v208, v58, s[40:43], s93 offen offset:0 sc1\n"
      "s_mov_b32 s93, 1726464\n"
      "buffer_store_dword v208, v58, s[40:43], s93 offen offset:0 sc1\n"
      "L_noreset:\n"
      "s_mov_b32 s60, s22\n"
      "s_and_b32 s61, s23, 0xffff\n"
      "s_mov_b32 s62, 1409024\n"
      "s_mov_b32 s63, 0x20000\n"
      "s_mov_b32 s52, s16\n"
      "s_and_b32 s53, s17, 0xffff\n"
      "s_mov_b32 s54, 4194304\n"
      "s_mov_b32 s55, 0x20000\n"
      "s_mov_b32 s36, s12\n"
      "s_and_b32 s37, s13, 0xffff\n"
      "s_mov_b32 s38, 67108864\n"
      "s_mov_b32 s39, 0x20000\n"
      "s_mov_b32 s44, s26\n"
      "s_and_b32 s45, s27, 0xffff\n"
      "s_mov_b32 s46, 2048\n"
      "s_mov_b32 s47, 0x20000\n"
      "s_mul_i32 s93, s77, 7\n"
      "s_add_u32 s4, s70, s93\n"
      "s_add_u32 m0, s92, 23936\n"
      "s_xor_b32 s85, s86, 69632\n"
      "buffer_load_dwordx4 v50, s[32:35], s85 offen nt lds\n"
      "s_add_u32 m0, s92, 25024\n"
      "s_xor_b32 s85, s86, 77824\n"
      "buffer_load_dwordx4 v50, s[32:35], s85 offen nt lds\n"
      "v_and_b32 v9, 1, v8\n"
      "v_lshlrev_b32 v10, 1, v9\n"
      "v_xor_b32 v10, v7, v10\n"
      "v_lshl_or_b32 v51, v10, 4, v51\n"
      "v_lshrrev_b32 v10, 2, v7\n"
      "v_mul_u32_u24 v10, 1088, v10\n"
      "v_add_u32 v10, s92, v10\n"
      "v_and_b32 v11, 3, v7\n"
      "v_xor_b32 v12, v8, v11\n"
      "v_lshl_add_u32 v2, v11, 8, v10\n"
      "v_lshl_add_u32 v2, v12, 4, v2\n"
      "v_lshl_add_u32 v3, v8, 8, v10\n"
      "v_lshl_add_u32 v3, v9, 5, v3\n"
      "v_lshl_add_u32 v3, v11, 3, v3\n"
      "v_xor_b32 v4, 32, v3\n"
      "s_cmp_eq_u32 s91, 0\n"
      "s_cselect_b32 s97, 0x80000000, 0\n"
      "s_lshl_b32 s93, s88, 5\n"
      "s_or_b32 s93, s93, s97\n"
      "v_lshrrev_b32 v9, 1, v0\n"
      "v_mul_u32_u24 v9, 2752, v9\n"
      "v_and_b32 v10, 1, v0\n"
      "v_lshlrev_b32 v10, 4, v10\n"
      "v_add3_u32 v47, v9, v10, s93\n"
      "s_lshl_b32 s93, s88, 6\n"
      "s_add_u32 s93, s93, s77\n"
      "s_lshl_b32 s93, s93, 8\n"
      "s_or_b32 s93, s93, s97\n"
      "v_lshl_add_u32 v9, v7, 4, s93\n"
      "v_lshl_add_u32 v46, v8, 10, v9\n"
      "L_pace8:\n"
      "s_memrealtime s[30:31]\n"
      "s_waitcnt lgkmcnt(0)\n"
      "s_sub_u32 s31, s30, s4\n"
      "s_cmpk_lt_i32 s31, 182\n"
      "s_cbranch_scc1 L_pace8\n"
      "s_cmpk_ge_i32 s31, 282\n"
      "s_cselect_b64 vcc, -1, 0\n"
      "s_cbranch_vccnz L_go8\n"
      "s_waitcnt vmcnt(15)\n"
      "s_cmpk_ge_i32 s31, 222\n"
      "s_cbranch_scc1 L_go8\n"
      "s_waitcnt vmcnt(14)\n"
      "L_go8:\n"
      "s_add_u32 m0, s92, 26112\n"
      "s_xor_b32 s85, s86, 32768\n"
      "buffer_load_dwordx4 v50, s[32:35], s85 offen nt lds\n"
      "s_waitcnt vmcnt(19)\n"
      "s_cbranch_vccnz L_go9\n"
      "s_waitcnt vmcnt(15)\n"
      "s_cmpk_ge_i32 s31, 222\n"
      "s_cbranch_scc1 L_go9\n"
      "s_waitcnt vmcnt(14)\n"
      "L_go9:\n"
      "s_add_u32 m0, s92, 27200\n"
      "s_xor_b32 s85, s86, 40960\n"
      "buffer_load_dwordx4 v50, s[32:35], s85 offen nt lds\n"
      "s_waitcnt vmcnt(19)\n"
      "s_cmp_lg_u32 s77, 0\n"
      "s_cbranch_scc1 L_et_b\n"
      "s_call_b64 s[66:67], L_stub_b\n"
      "L_et_b:\n"
      "L_pace10:\n"
      "s_memrealtime s[30:31]\n"
      "s_waitcnt lgkmcnt(0)\n"
      "s_sub_u32 s31, s30, s4\n"
      "s_cmpk_lt_i32 s31, 209\n"
      "s_cbranch_scc1 L_pace10\n"
      "s_cmpk_ge_i32 s31, 309\n"
      "s_cselect_b64 vcc, -1, 0\n"
      "s_cbranch_vccnz L_go10\n"
      "s_waitcnt vmcnt(15)\n"
      "s_cmpk_ge_i32 s31, 249\n"
      "s_cbranch_scc1 L_go10\n"
      "s_waitcnt vmcnt(14)\n"
      "L_go10:\n"
      "s_add_u32 m0, s92, 28288\n"
      "s_xor_b32 s85, s86, 98304\n"
      "buffer_load_dwordx4 v50, s[32:35], s85 offen nt lds\n"
      "s_waitcnt vmcnt(18)\n"
      "s_lshl_b32 s95, s76, 2\n"
      "s_add_u32 s95, s95, s77\n"
      "s_mul_i32 s95, s95, 960\n"
      "s_add_u32 s95, s95, 262144\n"
      "v_add_u32 v9, 4, v0\n"
      "v_and_b32 v9, 64, v9\n"
      "v_lshlrev_b32 v9, 25, v9\n"
      "v_lshl_or_b32 v9, v0, 4, v9\n"
      "buffer_store_dwordx4 v[20:23], v9, s[40:43], s95 offen sc1\n"
      "s_cbranch_vccnz L_go11\n"
      "s_waitcnt vmcnt(16)\n"
      "s_cmpk_ge_i32 s31, 249\n"
      "s_cbranch_scc1 L_go11\n"
      "s_waitcnt vmcnt(5)\n"
      "L_go11:\n"
      "s_add_u32 m0, s92, 29376\n"
      "s_xor_b32 s85, s86, 106496\n"
      "buffer_load_dwordx4 v50, s[32:35], s85 offen nt lds\n"
      "s_waitcnt vmcnt(19)\n"
      "ds_read_b128 a[0:3], v2 offset:17408\n"
      "ds_read_b128 a[4:7], v2 offset:17472\n"
      "ds_read_b128 a[8:11], v2 offset:17536\n"
      "ds_read_b128 a[12:15], v2 offset:17600\n"
      "L_pace12:\n"
      "s_memrealtime s[30:31]\n"
      "s_waitcnt lgkmcnt(0)\n"
      "s_sub_u32 s31, s30, s4\n"
      "s_cmpk_lt_i32 s31, 250\n"
      "s_cbranch_scc1 L_pace12\n"
      "s_sub_u32 s93, s31, 280\n"
      "s_max_i32 s93, s93, 0\n"
      "s_add_u32 s4, s4, s93\n"
      "s_sub_u32 s31, s31, s93\n"
      "s_cmpk_ge_i32 s31, 350\n"
      "s_cselect_b64 vcc, -1, 0\n"
      "s_cbranch_vccnz L_go12\n"
      "s_waitcnt vmcnt(6)\n"
      "s_cmpk_ge_i32 s31, 290\n"
      "s_cbranch_scc1 L_go12\n"
      "s_waitcnt vmcnt(5)\n"
      "L_go12:\n"
      "s_add_u32 m0, s92, 30464\n"
      "s_xor_b32 s85, s86, 36864\n"
      "buffer_load_dwordx4 v50, s[32:35], s85 offen nt lds\n"
      "s_waitcnt vmcnt(19)\n"
      "s_cbranch_vccnz L_go13\n"
      "s_waitcnt vmcnt(6)\n"
      "s_cmpk_ge_i32 s31, 290\n"
      "s_cbranch_scc1 L_go13\n"
      "s_waitcnt vmcnt(5)\n"
      "L_go13:\n"
      "s_add_u32 m0, s92, 31552\n"
      "s_xor_b32 s85, s86, 45056\n"
      "buffer_load_dwordx4 v50, s[32:35], s85 offen nt lds\n"
      "s_waitcnt vmcnt(19)\n"
      "L_pace14:\n"
      "s_memrealtime s[30:31]\n"
      "s_waitcnt lgkmcnt(0)\n"
      "s_sub_u32 s31, s30, s4\n"
      "s_cmpk_lt_i32 s31, 277\n"
      "s_cbranch_scc1 L_pace14\n"
      "s_cmpk_ge_i32 s31, 377\n"
      "s_cselect_b64 vcc, -1, 0\n"
      "s_cbranch_vccnz L_go14\n"
      "s_waitcnt vmcnt(6)\n"
      "s_cmpk_ge_i32 s31, 317\n"
      "s_cbranch_scc1 L_go14\n"
      "s_waitcnt vmcnt(5)\n"
      "L_go14:\n"
      "s_add_u32 m0, s92, 32640\n"
      "s_xor_b32 s85, s86, 102400\n"
      "buffer_load_dwordx4 v50, s[32:35], s85 offen nt lds\n"
      "s_waitcnt vmcnt(9)\n"
      "s_cbranch_vccnz L_go15\n"
      "s_waitcnt vmcnt(6)\n"
      "s_cmpk_ge_i32 s31, 317\n"
      "s_cbranch_scc1 L_go15\n"
      "s_waitcnt vmcnt(5)\n"
      "L_go15:\n"
      "s_add_u32 m0, s92, 33728\n"
      "s_xor_b32 s85, s86, 110592\n"
      "buffer_load_dwordx4 v50, s[32:35], s85 offen nt lds\n"
      "s_waitcnt vmcnt(9)\n"
      "ds_read_b128 a[16:19], v2 offset:21760\n"
      "ds_read_b128 a[20:23], v2 offset:21824\n"
      "ds_read_b128 a[24:27], v2 offset:21888\n"
      "ds_read_b128 a[28:31], v2 offset:21952\n"
      "v_mul_u32_u24 v9, 1088, v8\n"
      "v_add_u32 v9, s92, v9\n"
      "v_lshrrev_b32 v10, 2, v7\n"
      "v_lshl_add_u32 v9, v10, 7, v9\n"
      "v_and_b32 v10, 3, v7\n"
      "v_lshl_add_u32 v9, v10, 3, v9\n"
      "ds_read_b64_tr_b16 v[80:81], v9 offset:0\n"
      "ds_read_b64_tr_b16 v[82:83], v9 offset:512\n"
      "ds_read_b64_tr_b16 v[84:85], v9 offset:32\n"
      "ds_read_b64_tr_b16 v[86:87], v9 offset:544\n"
      "ds_read_b64_tr_b16 v[88:89], v9 offset:64\n"
      "ds_read_b64_tr_b16 v[90:91], v9 offset:576\n"
      "ds_read_b64_tr_b16 v[92:93], v9 offset:96\n"
      "ds_read_b64_tr_b16 v[94:95], v9 offset:608\n"
      "s_lshr_b32 s93, s90, 1\n"
      "s_mul_i32 s96, s93, 57344\n"
      "s_and_b32 s95, s90, 1\n"
      "s_lshl_b32 s65, s95, 11\n"
      "s_lshl_b32 s95, s95, 1\n"
      "s_lshr_b32 s97, s77, 1\n"
      "s_add_u32 s95, s95, s97\n"
      "s_lshl_b32 s64, s95, 13\n"
      "s_add_u32 s64, s64, s96\n"
      "s_and_b32 s97, s77, 1\n"
      "s_lshl_b32 s97, s97, 11\n"
      "s_add_u32 s64, s64, s97\n"
      "s_lshl_b32 s97, s89, 6\n"
      "s_add_u32 s64, s64, s97\n"
      "s_add_u32 s64, s64, 1261568\n"
      "s_add_u32 s65, s65, s96\n"
      "s_add_u32 s65, s65, s97\n"
      "s_add_u32 s65, s65, s97\n"
      "s_add_u32 s65, s65, 1310720\n"
      "v_mov_b32 v16, 0x3f803f80\n"
      "v_mov_b32 v17, 0x3f803f80\n"
      "v_mov_b32 v18, 0x3f803f80\n"
      "v_mov_b32 v19, 0x3f803f80\n"
      "s_lshl_b32 s93, s89, 6\n"
      "s_add_u32 s85, s93, 1245184\n"
      "v_and_b32 v12, 7, v0\n"
      "v_lshlrev_b32 v12, 3, v12\n"
      "v_and_b32 v13, 8, v0\n"
      "v_lshl_add_u32 v12, v13, 7, v12\n"
      "v_lshrrev_b32 v14, 4, v0\n"
      "v_lshlrev_b32 v15, 12, v14\n"
      "s_lshl_b32 s97, s95, 12\n"
      "s_sub_u32 s97, 32768, s97\n"
      "v_mov_b32 v11, s97\n"
      "s_nop 1\n"
      "v_mov_b32_dpp v15, v11 quad_perm:[0,1,2,3] row_mask:0x4 bank_mask:0xf\n"
      "v_mov_b32 v11, 0x80000000\n"
      "v_add_u32 v12, v15, v12\n"
      "v_lshrrev_b32 v15, 1, v13\n"
      "v_xor_b32 v15, 4, v15\n"
      "v_mov_b32_dpp v12, v11 quad_perm:[0,1,2,3] row_mask:0x8 bank_mask:0xf\n"
      "v_lshl_add_u32 v15, v14, 3, v15\n"
      "s_cmp_eq_u32 s77, 0\n"
      "s_cselect_b32 s98, 0x01100101, 0\n"
      "s_mov_b32 s99, 0\n"
      "v_lshlrev_b32 v14, 4, v14\n"
      "v_lshl_add_u32 v14, v13, 10, v14\n"
      "v_cndmask_b32 v13, v11, v15, s[98:99]\n"
      "v_and_b32 v15, 4, v0\n"
      "v_lshl_add_u32 v14, v15, 8, v14\n"
      "v_and_b32 v15, 3, v0\n"
      "v_lshl_add_u32 v14, v15, 2, v14\n"
      "s_lshl_b32 s95, s90, 2\n"
      "s_add_u32 s95, s95, s77\n"
      "s_cmp_eq_u32 s95, 1\n"
      "s_cselect_b32 s95, 0, 0x80000000\n"
      "v_or_b32 v14, s95, v14\n"
      "s_waitcnt lgkmcnt(0)\n"
      "v_mov_b32_dpp v16, v32 quad_perm:[0,0,0,0] row_mask:0xf bank_mask:0x3\n"
      "v_mov_b32_dpp v17, v32 quad_perm:[1,1,1,1] row_mask:0xf bank_mask:0x3\n"
      "v_mov_b32_dpp v18, v32 quad_perm:[2,2,2,2] row_mask:0xf bank_mask:0x3\n"
      "v_mov_b32_dpp v19, v32 quad_perm:[3,3,3,3] row_mask:0xf bank_mask:0x3\n"
      "v_min_u32 v32, -2, v32\n"
      "s_nop 0\n"
      "v_mfma_f32_16x16x32_bf16 v[64:67], v[16:19], v[80:83], 0\n"
      "v_mfma_f32_16x16x32_bf16 v[68:71], v[16:19], v[84:87], 0\n"
      "v_mfma_f32_16x16x32_bf16 v[72:75], v[16:19], v[88:91], 0\n"
      "v_mfma_f32_16x16x32_bf16 v[76:79], v[16:19], v[92:95], 0\n"
      "v_mfma_f32_16x16x32_bf16 v[60:63], v[16:19], v[16:19], 0\n"
      "s_nop 1\n"
      "v_mov_b32 v0, v0\n"
      "s_nop 1\n"
      "v_cvt_pk_bf16_f32 v20, v64, v68\n"
      "v_min_u32 v20, -2, v20\n"
      "v_cvt_pk_bf16_f32 v21, v72, v76\n"
      "v_min_u32 v21, -2, v21\n"
      "buffer_store_dwordx2 v[20:21], v12, s[40:43], s64 offen sc1\n"
      "v_min_u32 v60, -2, v60\n"
      "buffer_store_dword v60, v13, s[40:43], s65 offen sc1\n"
      "buffer_store_dword v32, v14, s[40:43], s85 offen sc1\n"
      "L_pace16:\n"
      "s_memrealtime s[30:31]\n"
      "s_waitcnt lgkmcnt(0)\n"
      "s_sub_u32 s31, s30, s4\n"
      "s_cmpk_lt_i32 s31, 321\n"
      "s_cbranch_scc1 L_pace16\n"
      "s_sub_u32 s93, s31, 351\n"
      "s_max_i32 s93, s93, 0\n"
      "s_add_u32 s4, s4, s93\n"
      "s_sub_u32 s31, s31, s93\n"
      "s_cmpk_ge_i32 s31, 421\n"
      "s_cselect_b64 vcc, -1, 0\n"
      "s_cbranch_vccnz L_go16\n"
      "s_waitcnt vmcnt(9)\n"
      "s_cmpk_ge_i32 s31, 361\n"
      "s_cbranch_scc1 L_go16\n"
      "s_waitcnt vmcnt(7)\n"
      "L_go16:\n"
      "s_add_u32 m0, s92, 17408\n"
      "s_xor_b32 s85, s86, 131072\n"
      "buffer_load_dwordx4 v50, s[32:35], s85 offen nt lds\n"
      "s_waitcnt vmcnt(12)\n"
      "s_cbranch_vccnz L_go17\n"
      "s_waitcnt vmcnt(8)\n"
      "s_cmpk_ge_i32 s31, 361\n"
      "s_cbranch_scc1 L_go17\n"
      "s_waitcnt vmcnt(7)\n"
      "L_go17:\n"
      "s_add_u32 m0, s92, 18496\n"
      "s_xor_b32 s85, s86, 139264\n"
      "buffer_load_dwordx4 v50, s[32:35], s85 offen nt lds\n"
      "s_waitcnt vmcnt(12)\n"
      "L_pace18:\n"
      "s_memrealtime s[30:31]\n"
      "s_waitcnt lgkmcnt(0)\n"
      "s_sub_u32 s31, s30, s4\n"
      "s_cmpk_lt_i32 s31, 348\n"
      "s_cbranch_scc1 L_pace18\n"
      "s_cmpk_ge_i32 s31, 448\n"
      "s_cselect_b64 vcc, -1, 0\n"
      "s_cbranch_vccnz L_go18\n"
      "s_waitcnt vmcnt(8)\n"
      "s_cmpk_ge_i32 s31, 388\n"
      "s_cbranch_scc1 L_go18\n"
      "s_waitcnt vmcnt(7)\n"
      "L_go18:\n"
      "s_add_u32 m0, s92, 19584\n"
      "s_xor_b32 s85, s86, 196608\n"
      "buffer_load_dwordx4 v50, s[32:35], s85 offen nt lds\n"
      "s_waitcnt vmcnt(12)\n"
      "s_waitcnt vmcnt(11)\n"
      "s_cmp_lg_u32 s77, 0\n"
      "s_cbranch_scc1 L_noprobe0\n"
      "s_memtime s[64:65]\n"
      "s_waitcnt lgkmcnt(0)\n"
      "s_load_dword s65, s[68:69], 0\n"
      "s_waitcnt lgkmcnt(0)\n"
      "s_memtime s[66:67]\n"
      "s_waitcnt lgkmcnt(0)\n"
      "s_sub_u32 s72, s66, s64\n"
      "L_noprobe0:\n"
      "s_cbranch_vccnz L_go19\n"
      "s_waitcnt vmcnt(8)\n"
      "s_cmpk_ge_i32 s31, 388\n"
      "s_cbranch_scc1 L_go19\n"
      "s_waitcnt vmcnt(7)\n"
      "L_go19:\n"
      "s_add_u32 m0, s92, 20672\n"
      "s_xor_b32 s85, s86, 204800\n"
      "buffer_load_dwordx4 v50, s[32:35], s85 offen nt lds\n"
      "s_waitcnt vmcnt(11)\n"
      "ds_read_b128 a[32:35], v2 offset:26112\n"
      "ds_read_b128 a[36:39], v2 offset:26176\n"
      "ds_read_b128 a[40:43], v2 offset:26240\n"
      "ds_read_b128 a[44:47], v2 offset:26304\n"
      "L_pace20:\n"
      "s_memrealtime s[30:31]\n"
      "s_waitcnt lgkmcnt(0)\n"
      "s_sub_u32 s31, s30, s4\n"
      "s_cmpk_lt_i32 s31, 375\n"
      "s_cbranch_scc1 L_pace20\n"
      "s_sub_u32 s93, s31, 405\n"
      "s_max_i32 s93, s93, 0\n"
      "s_add_u32 s4, s4, s93\n"
      "s_sub_u32 s31, s31, s93\n"
      "s_cmpk_ge_i32 s31, 475\n"
      "s_cselect_b64 vcc, -1, 0\n"
      "s_cbranch_vccnz L_go20\n"
      "s_waitcnt vmcnt(8)\n"
      "s_cmpk_ge_i32 s31, 415\n"
      "s_cbranch_scc1 L_go20\n"
      "s_waitcnt vmcnt(7)\n"
      "L_go20:\n"
      "s_add_u32 m0, s92, 21760\n"
      "s_xor_b32 s85, s86, 135168\n"
      "buffer_load_dwordx4 v50, s[32:35], s85 offen nt lds\n"
      "s_cmp_lg_u32 s77, 0\n"
      "s_cbranch_scc1 L_w1_0_0\n"
      "s_mov_b32 m0, 139264\n"
      "s_mov_b32 s85, 0\n"
      "buffer_load_dwordx4 v47, s[56:59], s85 offen   lds\n"
      "L_w1_0_0:\n"
      "s_waitcnt vmcnt(11)\n"
      "s_cbranch_vccnz L_go21\n"
      "s_waitcnt vmcnt(8)\n"
      "s_cmpk_ge_i32 s31, 415\n"
      "s_cbranch_scc1 L_go21\n"
      "s_waitcnt vmcnt(4)\n"
      "L_go21:\n"
      "s_add_u32 m0, s92, 22848\n"
      "s_xor_b32 s85, s86, 143360\n"
      "buffer_load_dwordx4 v50, s[32:35], s85 offen nt lds\n"
      "s_waitcnt vmcnt(11)\n"
      "s_cmp_lg_u32 s77, 0\n"
      "s_cbranch_scc1 L_noprobe1\n"
      "s_memtime s[64:65]\n"
      "s_waitcnt lgkmcnt(0)\n"
      "s_load_dword s65, s[68:69], 8192\n"
      "s_waitcnt lgkmcnt(0)\n"
      "s_memtime s[66:67]\n"
      "s_waitcnt lgkmcnt(0)\n"
      "s_sub_u32 s73, s66, s64\n"
      "s_cmp_gt_u32 s72, s73\n"
      "s_cselect_b32 s5, 1, 0\n"
      "s_lshr_b32 s93, s78, 2\n"
      "s_xor_b32 s5, s5, s93\n"
      "s_add_u32 s5, s5, 1\n"
      "L_noprobe1:\n"
      "L_pace22:\n"
      "s_memrealtime s[30:31]\n"
      "s_waitcnt lgkmcnt(0)\n"
      "s_sub_u32 s31, s30, s4\n"
      "s_cmpk_lt_i32 s31, 403\n"
      "s_cbranch_scc1 L_pace22\n"
      "s_cmpk_ge_i32 s31, 503\n"
      "s_cselect_b64 vcc, -1, 0\n"
      "s_cbranch_vccnz L_go22\n"
      "s_waitcnt vmcnt(5)\n"
      "s_cmpk_ge_i32 s31, 443\n"
      "s_cbranch_scc1 L_go22\n"
      "s_waitcnt vmcnt(4)\n"
      "L_go22:\n"
      "s_add_u32 m0, s92, 23936\n"
      "s_xor_b32 s85, s86, 200704\n"
      "buffer_load_dwordx4 v50, s[32:35], s85 offen nt lds\n"
      "s_add_u32 m0, s92, 4352\n"
      "s_mov_b32 s85, 0\n"
      "buffer_load_dwordx4 v46, s[60:63], s85 offen   lds\n"
      "s_waitcnt vmcnt(12)\n"
      "s_cbranch_vccnz L_go23\n"
      "s_waitcnt vmcnt(6)\n"
      "s_cmpk_ge_i32 s31, 443\n"
      "s_cbranch_scc1 L_go23\n"
      "s_waitcnt vmcnt(5)\n"
      "L_go23:\n"
      "s_add_u32 m0, s92, 25024\n"
      "s_xor_b32 s85, s86, 208896\n"
      "buffer_load_dwordx4 v50, s[32:35], s85 offen nt lds\n"
      "s_waitcnt vmcnt(12)\n"
      "ds_read_b128 a[48:51], v2 offset:30464\n"
      "ds_read_b128 a[52:55], v2 offset:30528\n"
      "ds_read_b128 a[56:59], v2 offset:30592\n"
      "ds_read_b128 a[60:63], v2 offset:30656\n"
      "L_pace24:\n"
      "s_memrealtime s[30:31]\n"
      "s_waitcnt lgkmcnt(0)\n"
      "s_sub_u32 s31, s30, s4\n"
      "s_cmpk_lt_i32 s31, 437\n"
      "s_cbranch_scc1 L_pace24\n"
      "s_cmpk_ge_i32 s31, 537\n"
      "s_cselect_b64 vcc, -1, 0\n"
      "s_cbranch_vccnz L_go24\n"
      "s_waitcnt vmcnt(6)\n"
      "s_cmpk_ge_i32 s31, 477\n"
      "s_cbranch_scc1 L_go24\n"
      "s_waitcnt vmcnt(5)\n"
      "L_go24:\n"
      "s_add_u32 m0, s92, 26112\n"
      "s_xor_b32 s85, s86, 163840\n"
      "buffer_load_dwordx4 v50, s[32:35], s85 offen nt lds\n"
      "s_cmp_lg_u32 s77, 1\n"
      "s_cbranch_scc1 L_w1_1_0\n"
      "s_mov_b32 m0, 143360\n"
      "s_mov_b32 s85, 352256\n"
      "buffer_load_dwordx4 v47, s[56:59], s85 offen   lds\n"
      "L_w1_1_0:\n"
      "s_waitcnt vmcnt(9)\n"
      "s_barrier\n"
      "s_lshl_b32 s94, s4, 12\n"
      "s_or_b32 s94, s94, s5\n"
      "v_mov_b32 v56, s94\n"
      "buffer_store_dword v56, v57, s[40:43], s7 offen sc1\n"
      "s_cbranch_vccnz L_go25\n"
      "s_waitcnt vmcnt(7)\n"
      "s_cmpk_ge_i32 s31, 477\n"
      "s_cbranch_scc1 L_go25\n"
      "s_waitcnt vmcnt(6)\n"
      "L_go25:\n"
      "s_add_u32 m0, s92, 27200\n"
      "s_xor_b32 s85, s86, 172032\n"
      "buffer_load_dwordx4 v50, s[32:35], s85 offen nt lds\n"
      "s_branch L_over_k26\n"
      "L_pend_k26:\n"
      ".p2align 12\n"
      "L_stub_k26:\n"
      "s_setpc_b64 s[66:67]\n"
      "L_over_k26:\n"
      "s_waitcnt vmcnt(10)\n"
      "L_pace26:\n"
      "s_memrealtime s[30:31]\n"
      "s_waitcnt lgkmcnt(0)\n"
      "s_sub_u32 s31, s30, s4\n"
      "s_cmpk_lt_i32 s31, 464\n"
      "s_cbranch_scc1 L_pace26\n"
      "s_cmpk_ge_i32 s31, 564\n"
      "s_cselect_b64 vcc, -1, 0\n"
      "s_cbranch_vccnz L_go26\n"
      "s_waitcnt vmcnt(7)\n"
      "s_cmpk_ge_i32 s31, 504\n"
      "s_cbranch_scc1 L_go26\n"
      "s_waitcnt vmcnt(6)\n"
      "L_go26:\n"
      "s_add_u32 m0, s92, 28288\n"
      "s_xor_b32 s85, s86, 229376\n"
      "buffer_load_dwordx4 v50, s[32:35], s85 offen nt lds\n"
      "s_waitcnt vmcnt(10)\n"
      "s_cbranch_vccnz L_go27\n"
      "s_waitcnt vmcnt(7)\n"
      "s_cmpk_ge_i32 s31, 504\n"
      "s_cbranch_scc1 L_go27\n"
      "s_waitcnt vmcnt(6)\n"
      "L_go27:\n"
      "s_add_u32 m0, s92, 29376\n"
      "s_xor_b32 s85, s86, 237568\n"
      "buffer_load_dwordx4 v50, s[32:35], s85 offen nt lds\n"
      "s_add_u32 m0, s92, 5440\n"
      "s_mov_b32 s85, 4096\n"
      "buffer_load_dwordx4 v46, s[60:63], s85 offen   lds\n"
      "s_waitcnt vmcnt(11)\n"
      "ds_read_b128 a[64:67], v2 offset:17408\n"
      "ds_read_b128 a[68:71], v2 offset:17472\n"
      "ds_read_b128 a[72:75], v2 offset:17536\n"
      "ds_read_b128 a[76:79], v2 offset:17600\n"
      "L_pace28:\n"
      "s_memrealtime s[30:31]\n"
      "s_waitcnt lgkmcnt(0)\n"
      "s_sub_u32 s31, s30, s4\n"
      "s_cmpk_lt_i32 s31, 498\n"
      "s_cbranch_scc1 L_pace28\n"
      "s_cmpk_ge_i32 s31, 598\n"
      "s_cselect_b64 vcc, -1, 0\n"
      "s_cbranch_vccnz L_go28\n"
      "s_waitcnt vmcnt(8)\n"
      "s_cmpk_ge_i32 s31, 538\n"
      "s_cbranch_scc1 L_go28\n"
      "s_waitcnt vmcnt(6)\n"
      "L_go28:\n"
      "s_add_u32 m0, s92, 30464\n"
      "s_xor_b32 s85, s86, 167936\n"
      "buffer_load_dwordx4 v50, s[32:35], s85 offen nt lds\n"
      "s_cmp_lg_u32 s77, 2\n"
      "s_cbranch_scc1 L_w1_2_0\n"
      "s_mov_b32 m0, 147456\n"
      "s_mov_b32 s85, 704512\n"
      "buffer_load_dwordx4 v47, s[56:59], s85 offen   lds\n"
      "L_w1_2_0:\n"
      "s_waitcnt vmcnt(11)\n"
      "v_and_b32 v9, 1, v0\n"
      "v_mul_u32_u24 v1, 0xaaaa, v9\n"
      "v_add_u32 v1, 0x4444, v1\n"
      "s_lshl_b32 s93, s77, 9\n"
      "s_add_u32 s93, s93, 155648\n"
      "v_lshlrev_b32 v5, 4, v9\n"
      "v_lshl_add_u32 v5, v8, 5, v5\n"
      "v_add_u32 v5, s93, v5\n"
      "v_lshlrev_b32 v9, 4, v9\n"
      "v_mov_b32 v10, 0x3f80\n"
      "v_lshlrev_b32 v9, v9, v10\n"
      "v_bfe_u32 v10, v0, 1, 2\n"
      "v_cmp_eq_u32 vcc, 0, v10\n"
      "s_nop 1\n"
      "v_cndmask_b32 v16, 0, v9, vcc\n"
      "v_cmp_eq_u32 vcc, 1, v10\n"
      "s_nop 1\n"
      "v_cndmask_b32 v17, 0, v9, vcc\n"
      "v_cmp_eq_u32 vcc, 2, v10\n"
      "s_nop 1\n"
      "v_cndmask_b32 v18, 0, v9, vcc\n"
      "v_cmp_eq_u32 vcc, 3, v10\n"
      "s_nop 1\n"
      "v_cndmask_b32 v19, 0, v9, vcc\n"
      "v_and_b32 v10, 7, v0\n"
      "v_lshlrev_b32 v10, 4, v10\n"
      "v_add_u32 v10, 162752, v10\n"
      "ds_write_b128 v10, v[16:19]\n"
      "v_lshl_add_u32 v10, v8, 2, v10\n"
      "v_lshlrev_b32 v33, 16, v33\n"
      "ds_write_b32 v10, v33 offset:128\n"
      "v_lshlrev_b32 v10, 2, v7\n"
      "v_add_u32 v10, 163008, v10\n"
      "v_lshlrev_b32 v34, 16, v34\n"
      "ds_write_b32 v10, v34\n"
      "v_mov_b32 v12, 0\n"
      "v_lshlrev_b32 v11, 2, v0\n"
      "v_add_u32 v11, 160000, v11\n"
      "v_mov_b32 v10, 163392\n"
      "ds_write2_b32 v10, v12, v12 offset1:1\n"
      "ds_write_b32 v11, v12 offset:0\n"
      "ds_write_b32 v11, v12 offset:256\n"
      "ds_write_b32 v11, v12 offset:512\n"
      "ds_write_b32 v11, v12 offset:704\n"
      "ds_write_b32 v11, v12 offset:1280\n"
      "ds_write_b32 v11, v12 offset:1600\n"
      "ds_write_b32 v11, v12 offset:1920\n"
      "s_cbranch_vccnz L_go29\n"
      "s_waitcnt vmcnt(7)\n"
      "s_cmpk_ge_i32 s31, 538\n"
      "s_cbranch_scc1 L_go29\n"
      "s_waitcnt vmcnt(6)\n"
      "L_go29:\n"
      "s_add_u32 m0, s92, 31552\n"
      "s_xor_b32 s85, s86, 176128\n"
      "buffer_load_dwordx4 v50, s[32:35], s85 offen nt lds\n"
      "s_waitcnt vmcnt(11)\n"
      "L_pace30:\n"
      "s_memrealtime s[30:31]\n"
      "s_waitcnt lgkmcnt(0)\n"
      "s_sub_u32 s31, s30, s4\n"
      "s_cmpk_lt_i32 s31, 526\n"
      "s_cbranch_scc1 L_pace30\n"
      "s_cmpk_ge_i32 s31, 626\n"
      "s_cselect_b64 vcc, -1, 0\n"
      "s_cbranch_vccnz L_go30\n"
      "s_waitcnt vmcnt(7)\n"
      "s_cmpk_ge_i32 s31, 566\n"
      "s_cbranch_scc1 L_go30\n"
      "s_waitcnt vmcnt(5)\n"
      "L_go30:\n"
      "s_add_u32 m0, s92, 32640\n"
      "s_xor_b32 s85, s86, 233472\n"
      "buffer_load_dwordx4 v50, s[32:35], s85 offen nt lds\n"
      "s_waitcnt vmcnt(11)\n"
      "s_cbranch_vccnz L_go31\n"
      "s_waitcnt vmcnt(6)\n"
      "s_cmpk_ge_i32 s31, 566\n"
      "s_cbranch_scc1 L_go31\n"
      "s_waitcnt vmcnt(5)\n"
      "L_go31:\n"
      "s_add_u32 m0, s92, 33728\n"
      "s_xor_b32 s85, s86, 241664\n"
      "buffer_load_dwordx4 v50, s[32:35], s85 offen nt lds\n"
      "s_waitcnt vmcnt(10)\n"
      "ds_read_b128 a[80:83], v2 offset:21760\n"
      "ds_read_b128 a[84:87], v2 offset:21824\n"
      "ds_read_b128 a[88:91], v2 offset:21888\n"
      "ds_read_b128 a[92:95], v2 offset:21952\n"
      "L_pace32:\n"
      "s_memrealtime s[30:31]\n"
      "s_waitcnt lgkmcnt(0)\n"
      "s_sub_u32 s31, s30, s4\n"
      "s_cmpk_lt_i32 s31, 553\n"
      "s_cbranch_scc1 L_pace32\n"
      "s_cmpk_ge_i32 s31, 653\n"
      "s_cselect_b64 vcc, -1, 0\n"
      "s_cbranch_vccnz L_go32\n"
      "s_waitcnt vmcnt(6)\n"
      "s_cmpk_ge_i32 s31, 593\n"
      "s_cbranch_scc1 L_go32\n"
      "s_waitcnt vmcnt(5)\n"
      "L_go32:\n"
      "s_add_u32 m0, s92, 17408\n"
      "s_xor_b32 s85, s86, 1024\n"
      "buffer_load_dwordx4 v50, s[32:35], s85 offen nt lds\n"
      "s_cmp_lg_u32 s77, 3\n"
      "s_cbranch_scc1 L_w1_3_0\n"
      "s_mov_b32 m0, 151552\n"
      "s_mov_b32 s85, 1056768\n"
      "buffer_load_dwordx4 v47, s[56:59], s85 offen   lds\n"
      "L_w1_3_0:\n"
      "s_add_u32 m0, s92, 6528\n"
      "s_mov_b32 s85, 8192\n"
      "buffer_load_dwordx4 v46, s[60:63], s85 offen   lds\n"
      "s_waitcnt vmcnt(11)\n"
      "s_cbranch_vccnz L_go33\n"
      "s_waitcnt vmcnt(7)\n"
      "s_cmpk_ge_i32 s31, 593\n"
      "s_cbranch_scc1 L_go33\n"
      "s_waitcnt vmcnt(5)\n"
      "L_go33:\n"
      "s_add_u32 m0, s92, 18496\n"
      "s_xor_b32 s85, s86, 9216\n"
      "buffer_load_dwordx4 v50, s[32:35], s85 offen nt lds\n"
      "s_waitcnt vmcnt(10)\n"
      "L_pace34:\n"
      "s_memrealtime s[30:31]\n"
      "s_waitcnt lgkmcnt(0)\n"
      "s_sub_u32 s31, s30, s4\n"
      "s_cmpk_lt_i32 s31, 587\n"
      "s_cbranch_scc1 L_pace34\n"
      "s_cmpk_ge_i32 s31, 687\n"
      "s_cselect_b64 vcc, -1, 0\n"
      "s_cbranch_vccnz L_go34\n"
      "s_waitcnt vmcnt(6)\n"
      "s_cmpk_ge_i32 s31, 627\n"
      "s_cbranch_scc1 L_go34\n"
      "s_waitcnt vmcnt(5)\n"
      "L_go34:\n"
      "s_add_u32 m0, s92, 19584\n"
      "s_xor_b32 s85, s86, 66560\n"
      "buffer_load_dwordx4 v50, s[32:35], s85 offen nt lds\n"
      "s_waitcnt vmcnt(10)\n"
      "s_cmp_eq_u32 s6, 0\n"
      "s_cbranch_scc1 L_ld35\n"
      "s_waitcnt vmcnt(9)\n"
      "s_call_b64 s[66:67], L_lead\n"
      "L_ld35:\n"
      "s_cbranch_vccnz L_go35\n"
      "s_waitcnt vmcnt(6)\n"
      "s_cmpk_ge_i32 s31, 627\n"
      "s_cbranch_scc1 L_go35\n"
      "s_waitcnt vmcnt(5)\n"
      "L_go35:\n"
      "s_add_u32 m0, s92, 20672\n"
      "s_xor_b32 s85, s86, 74752\n"
      "buffer_load_dwordx4 v50, s[32:35], s85 offen nt lds\n"
      "s_waitcnt vmcnt(10)\n"
      "ds_read_b128 a[96:99], v2 offset:26112\n"
      "ds_read_b128 a[100:103], v2 offset:26176\n"
      "ds_read_b128 a[104:107], v2 offset:26240\n"
      "ds_read_b128 a[108:111], v2 offset:26304\n"
      "L_pace36:\n"
      "s_memrealtime s[30:31]\n"
      "s_waitcnt lgkmcnt(0)\n"
      "s_sub_u32 s31, s30, s4\n"
      "s_cmpk_lt_i32 s31, 614\n"
      "s_cbranch_scc1 L_pace36\n"
      "s_cmpk_ge_i32 s31, 714\n"
      "s_cselect_b64 vcc, -1, 0\n"
      "s_cbranch_vccnz L_go36\n"
      "s_waitcnt vmcnt(6)\n"
      "s_cmpk_ge_i32 s31, 654\n"
      "s_cbranch_scc1 L_go36\n"
      "s_waitcnt vmcnt(5)\n"
      "L_go36:\n"
      "s_add_u32 m0, s92, 21760\n"
      "s_xor_b32 s85, s86, 5120\n"
      "buffer_load_dwordx4 v50, s[32:35], s85 offen nt lds\n"
      "s_cmp_lg_u32 s77, 0\n"
      "s_cbranch_scc1 L_w1_0_1\n"
      "s_mov_b32 m0, 140288\n"
      "s_mov_b32 s85, 88064\n"
      "buffer_load_dwordx4 v47, s[56:59], s85 offen   lds\n"
      "L_w1_0_1:\n"
      "s_waitcnt vmcnt(9)\n"
      "s_cbranch_vccnz L_go37\n"
      "s_waitcnt vmcnt(6)\n"
      "s_cmpk_ge_i32 s31, 654\n"
      "s_cbranch_scc1 L_go37\n"
      "s_waitcnt vmcnt(5)\n"
      "L_go37:\n"
      "s_add_u32 m0, s92, 22848\n"
      "s_xor_b32 s85, s86, 13312\n"
      "buffer_load_dwordx4 v50, s[32:35], s85 offen nt lds\n"
      "s_add_u32 m0, s92, 7616\n"
      "s_mov_b32 s85, 12288\n"
      "buffer_load_dwordx4 v46, s[60:63], s85 offen   lds\n"
      "s_waitcnt vmcnt(10)\n"
      "L_pace38:\n"
      "s_memrealtime s[30:31]\n"
      "s_waitcnt lgkmcnt(0)\n"
      "s_sub_u32 s31, s30, s4\n"
      "s_cmpk_lt_i32 s31, 648\n"
      "s_cbranch_scc1 L_pace38\n"
      "s_cmpk_ge_i32 s31, 748\n"
      "s_cselect_b64 vcc, -1, 0\n"
      "s_cbranch_vccnz L_go38\n"
      "s_waitcnt vmcnt(7)\n"
      "s_cmpk_ge_i32 s31, 688\n"
      "s_cbranch_scc1 L_go38\n"
      "s_waitcnt vmcnt(5)\n"
      "L_go38:\n"
      "s_add_u32 m0, s92, 23936\n"
      "s_xor_b32 s85, s86, 70656\n"
      "buffer_load_dwordx4 v50, s[32:35], s85 offen nt lds\n"
      "s_waitcnt vmcnt(10)\n"
      "s_cbranch_vccnz L_go39\n"
      "s_waitcnt vmcnt(6)\n"
      "s_cmpk_ge_i32 s31, 688\n"
      "s_cbranch_scc1 L_go39\n"
      "s_waitcnt vmcnt(5)\n"
      "L_go39:\n"
      "s_add_u32 m0, s92, 25024\n"
      "s_xor_b32 s85, s86, 78848\n"
      "buffer_load_dwordx4 v50, s[32:35], s85 offen nt lds\n"
      "s_waitcnt vmcnt(10)\n"
      "ds_read_b128 a[112:115], v2 offset:30464\n"
      "ds_read_b128 a[116:119], v2 offset:30528\n"
      "ds_read_b128 a[120:123], v2 offset:30592\n"
      "ds_read_b128 a[124:127], v2 offset:30656\n"
      "L_pace40:\n"
      "s_memrealtime s[30:31]\n"
      "s_waitcnt lgkmcnt(0)\n"
      "s_sub_u32 s31, s30, s4\n"
      "s_cmpk_lt_i32 s31, 675\n"
      "s_cbranch_scc1 L_pace40\n"
      "s_cmpk_ge_i32 s31, 775\n"
      "s_cselect_b64 vcc, -1, 0\n"
      "s_cbranch_vccnz L_go40\n"
      "s_waitcnt vmcnt(6)\n"
      "s_cmpk_ge_i32 s31, 715\n"
      "s_cbranch_scc1 L_go40\n"
      "s_waitcnt vmcnt(5)\n"
      "L_go40:\n"
      "s_add_u32 m0, s92, 26112\n"
      "s_xor_b32 s85, s86, 33792\n"
      "buffer_load_dwordx4 v50, s[32:35], s85 offen nt lds\n"
      "s_cmp_lg_u32 s77, 1\n"
      "s_cbranch_scc1 L_w1_1_1\n"
      "s_mov_b32 m0, 144384\n"
      "s_mov_b32 s85, 440320\n"
      "buffer_load_dwordx4 v47, s[56:59], s85 offen   lds\n"
      "L_w1_1_1:\n"
      "s_waitcnt vmcnt(10)\n"
      "s_cbranch_vccnz L_go41\n"
      "s_waitcnt vmcnt(6)\n"
      "s_cmpk_ge_i32 s31, 715\n"
      "s_cbranch_scc1 L_go41\n"
      "s_waitcnt vmcnt(5)\n"
      "L_go41:\n"
      "s_add_u32 m0, s92, 27200\n"
      "s_xor_b32 s85, s86, 41984\n"
      "buffer_load_dwordx4 v50, s[32:35], s85 offen nt lds\n"
      "s_waitcnt vmcnt(9)\n"
      "L_pace42:\n"
      "s_memrealtime s[30:31]\n"
      "s_waitcnt lgkmcnt(0)\n"
      "s_sub_u32 s31, s30, s4\n"
      "s_cmpk_lt_i32 s31, 703\n"
      "s_cbranch_scc1 L_pace42\n"
      "s_cmpk_ge_i32 s31, 803\n"
      "s_cselect_b64 vcc, -1, 0\n"
      "s_cbranch_vccnz L_go42\n"
      "s_waitcnt vmcnt(6)\n"
      "s_cmpk_ge_i32 s31, 743\n"
      "s_cbranch_scc1 L_go42\n"
      "s_waitcnt vmcnt(5)\n"
      "L_go42:\n"
      "s_add_u32 m0, s92, 28288\n"
      "s_xor_b32 s85, s86, 99328\n"
      "buffer_load_dwordx4 v50, s[32:35], s85 offen nt lds\n"
      "s_waitcnt vmcnt(9)\n"
      "s_cbranch_vccnz L_go43\n"
      "s_waitcnt vmcnt(6)\n"
      "s_cmpk_ge_i32 s31, 743\n"
      "s_cbranch_scc1 L_go43\n"
      "s_waitcnt vmcnt(4)\n"
      "L_go43:\n"
      "s_add_u32 m0, s92, 29376\n"
      "s_xor_b32 s85, s86, 107520\n"
      "buffer_load_dwordx4 v50, s[32:35], s85 offen nt lds\n"
      "s_waitcnt vmcnt(9)\n"
      "ds_read_b128 a[128:131], v2 offset:17408\n"
      "ds_read_b128 a[132:135], v2 offset:17472\n"
      "ds_read_b128 a[136:139], v2 offset:17536\n"
      "ds_read_b128 a[140:143], v2 offset:17600\n"
      "L_pace44:\n"
      "s_memrealtime s[30:31]\n"
      "s_waitcnt lgkmcnt(0)\n"
      "s_sub_u32 s31, s30, s4\n"
      "s_cmpk_lt_i32 s31, 730\n"
      "s_cbranch_scc1 L_pace44\n"
      "s_cmpk_ge_i32 s31, 830\n"
      "s_cselect_b64 vcc, -1, 0\n"
      "s_cbranch_vccnz L_go44\n"
      "s_waitcnt vmcnt(5)\n"
      "s_cmpk_ge_i32 s31, 770\n"
      "s_cbranch_scc1 L_go44\n"
      "s_waitcnt vmcnt(4)\n"
      "L_go44:\n"
      "s_add_u32 m0, s92, 30464\n"
      "s_xor_b32 s85, s86, 37888\n"
      "buffer_load_dwordx4 v50, s[32:35], s85 offen nt lds\n"
      "s_cmp_lg_u32 s77, 2\n"
      "s_cbranch_scc1 L_w1_2_1\n"
      "s_mov_b32 m0, 148480\n"
      "s_mov_b32 s85, 792576\n"
      "buffer_load_dwordx4 v47, s[56:59], s85 offen   lds\n"
      "L_w1_2_1:\n"
      "s_waitcnt vmcnt(9)\n"
      "s_cmp_eq_u32 s6, 0\n"
      "s_cbranch_scc1 L_ld45\n"
      "s_waitcnt vmcnt(9)\n"
      "s_call_b64 s[66:67], L_lead\n"
      "L_ld45:\n"
      "s_cbranch_vccnz L_go45\n"
      "s_waitcnt vmcnt(5)\n"
      "s_cmpk_ge_i32 s31, 770\n"
      "s_cbranch_scc1 L_go45\n"
      "s_waitcnt vmcnt(4)\n"
      "L_go45:\n"
      "s_add_u32 m0, s92, 31552\n"
      "s_xor_b32 s85, s86, 46080\n"
      "buffer_load_dwordx4 v50, s[32:35], s85 offen nt lds\n"
      "s_waitcnt vmcnt(9)\n"
      "s_waitcnt vmcnt(8)\n"
      "v_lshl_add_u32 v9, v0, 2, s92\n"
      "s_waitcnt lgkmcnt(0)\n"
      "ds_read_b32 v64, v9 offset:4352\n"
      "ds_read_b32 v65, v9 offset:4608\n"
      "ds_read_b32 v66, v9 offset:4864\n"
      "ds_read_b32 v67, v9 offset:5120\n"
      "ds_read_b32 v68, v9 offset:5440\n"
      "ds_read_b32 v69, v9 offset:5696\n"
      "ds_read_b32 v70, v9 offset:5952\n"
      "ds_read_b32 v71, v9 offset:6208\n"
      "ds_read_b32 v72, v9 offset:6528\n"
      "ds_read_b32 v73, v9 offset:6784\n"
      "ds_read_b32 v74, v9 offset:7040\n"
      "ds_read_b32 v75, v9 offset:7296\n"
      "ds_read_b32 v76, v9 offset:7616\n"
      "ds_read_b32 v77, v9 offset:7872\n"
      "ds_read_b32 v78, v9 offset:8128\n"
      "ds_read_b32 v79, v9 offset:8384\n"
      "s_mov_b32 s93, 0x05040100\n"
      "s_mov_b32 s94, 0x07060302\n"
      "s_waitcnt lgkmcnt(0)\n"
      "v_perm_b32 v112, v65, v64, s93\n"
      "v_perm_b32 v113, v65, v64, s94\n"
      "v_perm_b32 v114, v67, v66, s93\n"
      "v_perm_b32 v115, v67, v66, s94\n"
      "v_perm_b32 v116, v69, v68, s93\n"
      "v_perm_b32 v117, v69, v68, s94\n"
      "v_perm_b32 v118, v71, v70, s93\n"
      "v_perm_b32 v119, v71, v70, s94\n"
      "v_perm_b32 v120, v73, v72, s93\n"
      "v_perm_b32 v121, v73, v72, s94\n"
      "v_perm_b32 v122, v75, v74, s93\n"
      "v_perm_b32 v123, v75, v74, s94\n"
      "v_perm_b32 v124, v77, v76, s93\n"
      "v_perm_b32 v125, v77, v76, s94\n"
      "v_perm_b32 v126, v79, v78, s93\n"
      "v_perm_b32 v127, v79, v78, s94\n"
      "L_pace46:\n"
      "s_memrealtime s[30:31]\n"
      "s_waitcnt lgkmcnt(0)\n"
      "s_sub_u32 s31, s30, s4\n"
      "s_cmpk_lt_i32 s31, 758\n"
      "s_cbranch_scc1 L_pace46\n"
      "s_cmpk_ge_i32 s31, 858\n"
      "s_cselect_b64 vcc, -1, 0\n"
      "s_cbranch_vccnz L_go46\n"
      "s_waitcnt vmcnt(5)\n"
      "s_cmpk_ge_i32 s31, 798\n"
      "s_cbranch_scc1 L_go46\n"
      "s_waitcnt vmcnt(4)\n"
      "L_go46:\n"
      "s_add_u32 m0, s92, 32640\n"
      "s_xor_b32 s85, s86, 103424\n"
      "buffer_load_dwordx4 v50, s[32:35], s85 offen nt lds\n"
      "s_waitcnt vmcnt(8)\n"
      "s_cbranch_vccnz L_go47\n"
      "s_waitcnt vmcnt(5)\n"
      "s_cmpk_ge_i32 s31, 798\n"
      "s_cbranch_scc1 L_go47\n"
      "s_waitcnt vmcnt(4)\n"
      "L_go47:\n"
      "s_add_u32 m0, s92, 33728\n"
      "s_xor_b32 s85, s86, 111616\n"
      "buffer_load_dwordx4 v50, s[32:35], s85 offen nt lds\n"
      "s_waitcnt vmcnt(8)\n"
      "ds_read_b128 a[144:147], v2 offset:21760\n"
      "ds_read_b128 a[148:151], v2 offset:21824\n"
      "ds_read_b128 a[152:155], v2 offset:21888\n"
      "ds_read_b128 a[156:159], v2 offset:21952\n"
      "L_pace48:\n"
      "s_memrealtime s[30:31]\n"
      "s_waitcnt lgkmcnt(0)\n"
      "s_sub_u32 s31, s30, s4\n"
      "s_cmpk_lt_i32 s31, 785\n"
      "s_cbranch_scc1 L_pace48\n"
      "s_cmpk_ge_i32 s31, 885\n"
      "s_cselect_b64 vcc, -1, 0\n"
      "s_cbranch_vccnz L_go48\n"
      "s_waitcnt vmcnt(5)\n"
      "s_cmpk_ge_i32 s31, 825\n"
      "s_cbranch_scc1 L_go48\n"
      "s_waitcnt vmcnt(4)\n"
      "L_go48:\n"
      "s_add_u32 m0, s92, 0\n"
      "s_xor_b32 s85, s86, 132096\n"
      "buffer_load_dwordx4 v50, s[32:35], s85 offen nt lds\n"
      "s_cmp_lg_u32 s77, 3\n"
      "s_cbranch_scc1 L_w1_3_1\n"
      "s_mov_b32 m0, 152576\n"
      "s_mov_b32 s85, 1144832\n"
      "buffer_load_dwordx4 v47, s[56:59], s85 offen   lds\n"
      "L_w1_3_1:\n"
      "s_waitcnt vmcnt(8)\n"
      "s_cbranch_vccnz L_go49\n"
      "s_waitcnt vmcnt(5)\n"
      "s_cmpk_ge_i32 s31, 825\n"
      "s_cbranch_scc1 L_go49\n"
      "s_waitcnt vmcnt(4)\n"
      "L_go49:\n"
      "s_add_u32 m0, s92, 1088\n"
      "s_xor_b32 s85, s86, 140288\n"
      "buffer_load_dwordx4 v50, s[32:35], s85 offen nt lds\n"
      "s_waitcnt vmcnt(8)\n"
      "L_pace50:\n"
      "s_memrealtime s[30:31]\n"
      "s_waitcnt lgkmcnt(0)\n"
      "s_sub_u32 s31, s30, s4\n"
      "s_cmpk_lt_i32 s31, 813\n"
      "s_cbranch_scc1 L_pace50\n"
      "s_cmpk_ge_i32 s31, 913\n"
      "s_cselect_b64 vcc, -1, 0\n"
      "s_cbranch_vccnz L_go50\n"
      "s_waitcnt vmcnt(5)\n"
      "s_cmpk_ge_i32 s31, 853\n"
      "s_cbranch_scc1 L_go50\n"
      "s_waitcnt vmcnt(4)\n"
      "L_go50:\n"
      "s_add_u32 m0, s92, 2176\n"
      "s_xor_b32 s85, s86, 197632\n"
      "buffer_load_dwordx4 v50, s[32:35], s85 offen nt lds\n"
      "s_waitcnt vmcnt(8)\n"
      "s_cbranch_vccnz L_go51\n"
      "s_waitcnt vmcnt(5)\n"
      "s_cmpk_ge_i32 s31, 853\n"
      "s_cbranch_scc1 L_go51\n"
      "s_waitcnt vmcnt(4)\n"
      "L_go51:\n"
      "s_add_u32 m0, s92, 3264\n"
      "s_xor_b32 s85, s86, 205824\n"
      "buffer_load_dwordx4 v50, s[32:35], s85 offen nt lds\n"
      "s_waitcnt vmcnt(8)\n"
      "ds_read_b128 a[160:163], v2 offset:26112\n"
      "ds_read_b128 a[164:167], v2 offset:26176\n"
      "ds_read_b128 a[168:171], v2 offset:26240\n"
      "ds_read_b128 a[172:175], v2 offset:26304\n"
      "L_pace52:\n"
      "s_memrealtime s[30:31]\n"
      "s_waitcnt lgkmcnt(0)\n"
      "s_sub_u32 s31, s30, s4\n"
      "s_cmpk_lt_i32 s31, 840\n"
      "s_cbranch_scc1 L_pace52\n"
      "s_cmpk_ge_i32 s31, 940\n"
      "s_cselect_b64 vcc, -1, 0\n"
      "s_cbranch_vccnz L_go52\n"
      "s_waitcnt vmcnt(5)\n"
      "s_cmpk_ge_i32 s31, 880\n"
      "s_cbranch_scc1 L_go52\n"
      "s_waitcnt vmcnt(4)\n"
      "L_go52:\n"
      "s_add_u32 m0, s92, 4352\n"
      "s_xor_b32 s85, s86, 136192\n"
      "buffer_load_dwordx4 v50, s[32:35], s85 offen nt lds\n"
      "s_cmp_lg_u32 s77, 0\n"
      "s_cbranch_scc1 L_w1_0_2\n"
      "s_mov_b32 m0, 141312\n"
      "s_mov_b32 s85, 176128\n"
      "buffer_load_dwordx4 v47, s[56:59], s85 offen   lds\n"
      "L_w1_0_2:\n"
      "s_waitcnt vmcnt(8)\n"
      "s_cbranch_vccnz L_go53\n"
      "s_waitcnt vmcnt(5)\n"
      "s_cmpk_ge_i32 s31, 880\n"
      "s_cbranch_scc1 L_go53\n"
      "s_waitcnt vmcnt(4)\n"
      "L_go53:\n"
      "s_add_u32 m0, s92, 5440\n"
      "s_xor_b32 s85, s86, 144384\n"
      "buffer_load_dwordx4 v50, s[32:35], s85 offen nt lds\n"
      "s_waitcnt vmcnt(8)\n"
      "L_pace54:\n"
      "s_memrealtime s[30:31]\n"
      "s_waitcnt lgkmcnt(0)\n"
      "s_sub_u32 s31, s30, s4\n"
      "s_cmpk_lt_i32 s31, 868\n"
      "s_cbranch_scc1 L_pace54\n"
      "s_cmpk_ge_i32 s31, 968\n"
      "s_cselect_b64 vcc, -1, 0\n"
      "s_cbranch_vccnz L_go54\n"
      "s_waitcnt vmcnt(5)\n"
      "s_cmpk_ge_i32 s31, 908\n"
      "s_cbranch_scc1 L_go54\n"
      "s_waitcnt vmcnt(4)\n"
      "L_go54:\n"
      "s_add_u32 m0, s92, 6528\n"
      "s_xor_b32 s85, s86, 201728\n"
      "buffer_load_dwordx4 v50, s[32:35], s85 offen nt lds\n"
      "s_waitcnt vmcnt(8)\n"
      "s_cbranch_vccnz L_go55\n"
      "s_waitcnt vmcnt(5)\n"
      "s_cmpk_ge_i32 s31, 908\n"
      "s_cbranch_scc1 L_go55\n"
      "s_waitcnt vmcnt(4)\n"
      "L_go55:\n"
      "s_add_u32 m0, s92, 7616\n"
      "s_xor_b32 s85, s86, 209920\n"
      "buffer_load_dwordx4 v50, s[32:35], s85 offen nt lds\n"
      "s_waitcnt vmcnt(8)\n"
      "ds_read_b128 a[176:179], v2 offset:30464\n"
      "ds_read_b128 a[180:183], v2 offset:30528\n"
      "ds_read_b128 a[184:187], v2 offset:30592\n"
      "ds_read_b128 a[188:191], v2 offset:30656\n"
      "L_pace56:\n"
      "s_memrealtime s[30:31]\n"
      "s_waitcnt lgkmcnt(0)\n"
      "s_sub_u32 s31, s30, s4\n"
      "s_cmpk_lt_i32 s31, 896\n"
      "s_cbranch_scc1 L_pace56\n"
      "s_cmpk_ge_i32 s31, 996\n"
      "s_cselect_b64 vcc, -1, 0\n"
      "s_cbranch_vccnz L_go56\n"
      "s_waitcnt vmcnt(5)\n"
      "s_cmpk_ge_i32 s31, 936\n"
      "s_cbranch_scc1 L_go56\n"
      "s_waitcnt vmcnt(4)\n"
      "L_go56:\n"
      "s_add_u32 m0, s92, 8704\n"
      "s_xor_b32 s85, s86, 164864\n"
      "buffer_load_dwordx4 v50, s[32:35], s85 offen nt lds\n"
      "s_cmp_lg_u32 s77, 1\n"
      "s_cbranch_scc1 L_w1_1_2\n"
      "s_mov_b32 m0, 145408\n"
      "s_mov_b32 s85, 528384\n"
      "buffer_load_dwordx4 v47, s[56:59], s85 offen   lds\n"
      "L_w1_1_2:\n"
      "s_waitcnt vmcnt(8)\n"
      "s_cbranch_vccnz L_go57\n"
      "s_waitcnt vmcnt(5)\n"
      "s_cmpk_ge_i32 s31, 936\n"
      "s_cbranch_scc1 L_go57\n"
      "s_waitcnt vmcnt(4)\n"
      "L_go57:\n"
      "s_add_u32 m0, s92, 9792\n"
      "s_xor_b32 s85, s86, 173056\n"
      "buffer_load_dwordx4 v50, s[32:35], s85 offen nt lds\n"
      "s_waitcnt vmcnt(8)\n"
      "L_pace58:\n"
      "s_memrealtime s[30:31]\n"
      "s_waitcnt lgkmcnt(0)\n"
      "s_sub_u32 s31, s30, s4\n"
      "s_cmpk_lt_i32 s31, 923\n"
      "s_cbranch_scc1 L_pace58\n"
      "s_cmpk_ge_i32 s31, 1023\n"
      "s_cselect_b64 vcc, -1, 0\n"
      "s_cbranch_vccnz L_go58\n"
      "s_waitcnt vmcnt(5)\n"
      "s_cmpk_ge_i32 s31, 963\n"
      "s_cbranch_scc1 L_go58\n"
      "s_waitcnt vmcnt(4)\n"
      "L_go58:\n"
      "s_add_u32 m0, s92, 10880\n"
      "s_xor_b32 s85, s86, 230400\n"
      "buffer_load_dwordx4 v50, s[32:35], s85 offen nt lds\n"
      "s_waitcnt vmcnt(8)\n"
      "s_cbranch_vccnz L_go59\n"
      "s_waitcnt vmcnt(5)\n"
      "s_cmpk_ge_i32 s31, 963\n"
      "s_cbranch_scc1 L_go59\n"
      "s_waitcnt vmcnt(4)\n"
      "L_go59:\n"
      "s_add_u32 m0, s92, 11968\n"
      "s_xor_b32 s85, s86, 238592\n"
      "buffer_load_dwordx4 v50, s[32:35], s85 offen nt lds\n"
      "s_waitcnt vmcnt(8)\n"
      "buffer_load_dword v59, off, s[40:43], s7 offset:4 sc1\n"
      "L_pace60:\n"
      "s_memrealtime s[30:31]\n"
      "s_waitcnt lgkmcnt(0)\n"
      "s_sub_u32 s31, s30, s4\n"
      "s_cmpk_lt_i32 s31, 951\n"
      "s_cbranch_scc1 L_pace60\n"
      "s_cmpk_ge_i32 s31, 1051\n"
      "s_cselect_b64 vcc, -1, 0\n"
      "s_cbranch_vccnz L_go60\n"
      "s_waitcnt vmcnt(6)\n"
      "s_cmpk_ge_i32 s31, 991\n"
      "s_cbranch_scc1 L_go60\n"
      "s_waitcnt vmcnt(5)\n"
      "L_go60:\n"
      "s_add_u32 m0, s92, 13056\n"
      "s_xor_b32 s85, s86, 168960\n"
      "buffer_load_dwordx4 v50, s[32:35], s85 offen nt lds\n"
      "s_cmp_lg_u32 s77, 2\n"
      "s_cbranch_scc1 L_w1_2_2\n"
      "s_mov_b32 m0, 149504\n"
      "s_mov_b32 s85, 880640\n"
      "buffer_load_dwordx4 v47, s[56:59], s85 offen   lds\n"
      "L_w1_2_2:\n"
      "s_waitcnt vmcnt(9)\n"
      "s_cbranch_vccnz L_go61\n"
      "s_waitcnt vmcnt(6)\n"
      "s_cmpk_ge_i32 s31, 991\n"
      "s_cbranch_scc1 L_go61\n"
      "s_waitcnt vmcnt(5)\n"
      "L_go61:\n"
      "s_add_u32 m0, s92, 14144\n"
      "s_xor_b32 s85, s86, 177152\n"
      "buffer_load_dwordx4 v50, s[32:35], s85 offen nt lds\n"
      "s_waitcnt vmcnt(9)\n"
      "L_pace62:\n"
      "s_memrealtime s[30:31]\n"
      "s_waitcnt lgkmcnt(0)\n"
      "s_sub_u32 s31, s30, s4\n"
      "s_cmpk_lt_i32 s31, 979\n"
      "s_cbranch_scc1 L_pace62\n"
      "s_cmpk_ge_i32 s31, 1079\n"
      "s_cselect_b64 vcc, -1, 0\n"
      "s_cbranch_vccnz L_go62\n"
      "s_waitcnt vmcnt(6)\n"
      "s_cmpk_ge_i32 s31, 1019\n"
      "s_cbranch_scc1 L_go62\n"
      "s_waitcnt vmcnt(5)\n"
      "L_go62:\n"
      "s_add_u32 m0, s92, 15232\n"
      "s_xor_b32 s85, s86, 234496\n"
      "buffer_load_dwordx4 v50, s[32:35], s85 offen nt lds\n"
      "s_waitcnt vmcnt(9)\n"
      "s_cbranch_vccnz L_go63\n"
      "s_waitcnt vmcnt(6)\n"
      "s_cmpk_ge_i32 s31, 1019\n"
      "s_cbranch_scc1 L_go63\n"
      "s_waitcnt vmcnt(5)\n"
      "L_go63:\n"
      "s_add_u32 m0, s92, 16320\n"
      "s_xor_b32 s85, s86, 242688\n"
      "buffer_load_dwordx4 v50, s[32:35], s85 offen nt lds\n"
      "s_waitcnt vmcnt(9)\n"
      "L_pace64:\n"
      "s_memrealtime s[30:31]\n"
      "s_waitcnt lgkmcnt(0)\n"
      "s_sub_u32 s31, s30, s4\n"
      "s_cmpk_lt_i32 s31, 1006\n"
      "s_cbranch_scc1 L_pace64\n"
      "s_cmpk_ge_i32 s31, 1106\n"
      "s_cselect_b64 vcc, -1, 0\n"
      "s_cbranch_vccnz L_go64\n"
      "s_waitcnt vmcnt(6)\n"
      "s_cmpk_ge_i32 s31, 1046\n"
      "s_cbranch_scc1 L_go64\n"
      "s_waitcnt vmcnt(5)\n"
      "L_go64:\n"
      "s_add_u32 m0, s92, 17408\n"
      "s_xor_b32 s85, s86, 0\n"
      "buffer_load_dwordx4 v51, s[36:39], s85 offen nt lds\n"
      "s_cmp_lg_u32 s77, 3\n"
      "s_cbranch_scc1 L_w1_3_2\n"
      "s_mov_b32 m0, 153600\n"
      "s_mov_b32 s85, 1232896\n"
      "buffer_load_dwordx4 v47, s[56:59], s85 offen   lds\n"
      "L_w1_3_2:\n"
      "s_waitcnt vmcnt(9)\n"
      "s_cbranch_vccnz L_go65\n"
      "s_waitcnt vmcnt(6)\n"
      "s_cmpk_ge_i32 s31, 1046\n"
      "s_cbranch_scc1 L_go65\n"
      "s_waitcnt vmcnt(4)\n"
      "L_go65:\n"
      "s_add_u32 m0, s92, 18496\n"
      "s_xor_b32 s85, s86, 4096\n"
      "buffer_load_dwordx4 v51, s[36:39], s85 offen nt lds\n"
      "s_branch L_over_k66\n"
      "L_pend_k66:\n"
      ".p2align 12\n"
      "L_stub_k66:\n"
      "s_setpc_b64 s[66:67]\n"
      "L_over_k66:\n"
      "s_waitcnt vmcnt(9)\n"
      "L_pace66:\n"
      "s_memrealtime s[30:31]\n"
      "s_waitcnt lgkmcnt(0)\n"
      "s_sub_u32 s31, s30, s4\n"
      "s_cmpk_lt_i32 s31, 1034\n"
      "s_cbranch_scc1 L_pace66\n"
      "s_cmpk_ge_i32 s31, 1134\n"
      "s_cselect_b64 vcc, -1, 0\n"
      "s_cbranch_vccnz L_go66\n"
      "s_waitcnt vmcnt(5)\n"
      "s_cmpk_ge_i32 s31, 1074\n"
      "s_cbranch_scc1 L_go66\n"
      "s_waitcnt vmcnt(4)\n"
      "L_go66:\n"
      "s_add_u32 m0, s92, 19584\n"
      "s_xor_b32 s85, s86, 8192\n"
      "buffer_load_dwordx4 v51, s[36:39], s85 offen nt lds\n"
      "s_waitcnt vmcnt(9)\n"
      "s_cbranch_vccnz L_go67\n"
      "s_waitcnt vmcnt(5)\n"
      "s_cmpk_ge_i32 s31, 1074\n"
      "s_cbranch_scc1 L_go67\n"
      "s_waitcnt vmcnt(4)\n"
      "L_go67:\n"
      "s_add_u32 m0, s92, 20672\n"
      "s_xor_b32 s85, s86, 12288\n"
      "buffer_load_dwordx4 v51, s[36:39], s85 offen nt lds\n"
      "s_waitcnt vmcnt(9)\n"
      "L_pace68:\n"
      "s_memrealtime s[30:31]\n"
      "s_waitcnt lgkmcnt(0)\n"
      "s_sub_u32 s31, s30, s4\n"
      "s_cmpk_lt_i32 s31, 1061\n"
      "s_cbranch_scc1 L_pace68\n"
      "s_cmpk_ge_i32 s31, 1161\n"
      "s_cselect_b64 vcc, -1, 0\n"
      "s_cbranch_vccnz L_go68\n"
      "s_waitcnt vmcnt(5)\n"
      "s_cmpk_ge_i32 s31, 1101\n"
      "s_cbranch_scc1 L_go68\n"
      "s_waitcnt vmcnt(4)\n"
      "L_go68:\n"
      "s_add_u32 m0, s92, 21760\n"
      "s_xor_b32 s85, s86, 65536\n"
      "buffer_load_dwordx4 v51, s[36:39], s85 offen nt lds\n"
      "s_cmp_lg_u32 s77, 0\n"
      "s_cbranch_scc1 L_w1_0_3\n"
      "s_mov_b32 m0, 142336\n"
      "s_mov_b32 s85, 264192\n"
      "buffer_load_dwordx4 v47, s[56:59], s85 offen   lds\n"
      "L_w1_0_3:\n"
      "s_waitcnt vmcnt(8)\n"
      "L_g_chk:\n"
      "v_readfirstlane_b32 s82, v59\n"
      "s_cmp_eq_u32 s82, 0\n"
      "s_cbranch_scc1 L_g_retry\n"
      "L_g_ok:\n"
      "s_mul_i32 s98, s78, 57344\n"
      "s_lshr_b32 s95, s79, 3\n"
      "s_lshl_b32 s72, s95, 13\n"
      "s_add_u32 s72, s72, s98\n"
      "s_bfe_u32 s97, s79, 0x10002\n"
      "s_lshl_b32 s73, s97, 3\n"
      "s_lshl_b32 s97, s97, 12\n"
      "s_add_u32 s72, s72, s97\n"
      "s_lshl_b32 s97, s77, 10\n"
      "s_add_u32 s72, s72, s97\n"
      "s_add_u32 s72, s72, 1261568\n"
      "s_add_u32 s87, s97, s98\n"
      "s_lshl_b32 s97, s95, 12\n"
      "s_add_u32 s87, s87, s97\n"
      "s_add_u32 s87, s87, 1294336\n"
      "s_lshr_b32 s95, s95, 1\n"
      "s_lshl_b32 s95, s95, 11\n"
      "s_add_u32 s73, s73, s95\n"
      "s_add_u32 s73, s73, s98\n"
      "s_add_u32 s73, s73, 1310720\n"
      "v_lshlrev_b32 v10, 4, v0\n"
      "v_lshrrev_b32 v11, 2, v0\n"
      "v_lshlrev_b32 v11, 7, v11\n"
      "v_and_b32 v15, 1, v0\n"
      "v_lshl_add_u32 v11, v15, 2, v11\n"
      "buffer_load_dword v13, v11, s[40:43], s73 offen sc1\n"
      "buffer_load_dwordx4 v[16:19], v10, s[40:43], s87 offen sc1\n"
      "buffer_load_dwordx4 v[32:35], v10, s[40:43], s72 offen sc1\n"
      "v_and_b32 v15, 7, v0\n"
      "v_lshlrev_b32 v15, 4, v15\n"
      "v_add_u32 v15, 162752, v15\n"
      "ds_read_b128 v[36:39], v15\n"
      "v_and_b32 v14, 3, v0\n"
      "v_lshlrev_b32 v14, 5, v14\n"
      "v_and_b32 v15, 16, v0\n"
      "v_add_u32 v14, v15, v14\n"
      "v_lshrrev_b32 v15, 5, v0\n"
      "v_lshl_add_u32 v14, v15, 2, v14\n"
      "v_bfe_u32 v15, v0, 2, 1\n"
      "v_lshl_add_u32 v14, v15, 1, v14\n"
      "s_lshr_b32 s95, s77, 1\n"
      "s_lshl_b32 s95, s95, 7\n"
      "s_and_b32 s97, s77, 1\n"
      "s_lshl_b32 s97, s97, 3\n"
      "s_add_u32 s95, s95, s97\n"
      "s_add_u32 s95, s95, 160960\n"
      "v_add_u32 v14, s95, v14\n"
      "s_cbranch_vccnz L_go69\n"
      "s_waitcnt vmcnt(8)\n"
      "s_cmpk_ge_i32 s31, 1101\n"
      "s_cbranch_scc1 L_go69\n"
      "s_waitcnt vmcnt(7)\n"
      "L_go69:\n"
      "s_add_u32 m0, s92, 22848\n"
      "s_xor_b32 s85, s86, 69632\n"
      "buffer_load_dwordx4 v51, s[36:39], s85 offen nt lds\n"
      "s_waitcnt vmcnt(11)\n"
      "L_pace70:\n"
      "s_memrealtime s[30:31]\n"
      "s_waitcnt lgkmcnt(0)\n"
      "s_sub_u32 s31, s30, s4\n"
      "s_cmpk_lt_i32 s31, 1102\n"
      "s_cbranch_scc1 L_pace70\n"
      "s_cmpk_ge_i32 s31, 1202\n"
      "s_cselect_b64 vcc, -1, 0\n"
      "s_cbranch_vccnz L_go70\n"
      "s_waitcnt vmcnt(8)\n"
      "s_cmpk_ge_i32 s31, 1142\n"
      "s_cbranch_scc1 L_go70\n"
      "s_waitcnt vmcnt(7)\n"
      "L_go70:\n"
      "s_add_u32 m0, s92, 23936\n"
      "s_xor_b32 s85, s86, 73728\n"
      "buffer_load_dwordx4 v51, s[36:39], s85 offen nt lds\n"
      "s_waitcnt vmcnt(11)\n"
      "s_lshl_b32 s93, s70, 9\n"
      "s_sub_u32 s93, s82, s93\n"
      "s_ashr_i32 s93, s93, 9\n"
      "s_add_u32 s70, s70, s93\n"
      "s_mul_i32 s93, s77, 7\n"
      "s_add_u32 s93, s93, s70\n"
      "s_add_u32 s93, s93, 0\n"
      "s_sub_u32 s93, s93, s4\n"
      "s_min_i32 s93, s93, 100\n"
      "s_add_u32 s4, s4, s93\n"
      "s_and_b32 s95, s78, 3\n"
      "s_lshr_b32 s97, s82, s95\n"
      "s_lshl_b32 s83, s95, 14\n"
      "s_lshr_b32 s95, s78, 2\n"
      "s_xor_b32 s97, s97, s95\n"
      "s_and_b32 s97, s97, 1\n"
      "s_lshl_b32 s97, s97, 13\n"
      "s_add_u32 s83, s83, s97\n"
      "s_mov_b32 s84, s97\n"
      "s_lshr_b32 s93, s90, 1\n"
      "s_and_b32 s95, s93, 3\n"
      "s_lshr_b32 s97, s82, s95\n"
      "s_lshl_b32 s96, s95, 14\n"
      "s_lshr_b32 s95, s93, 2\n"
      "s_xor_b32 s97, s97, s95\n"
      "s_and_b32 s97, s97, 1\n"
      "s_lshl_b32 s97, s97, 13\n"
      "s_add_u32 s96, s96, s97\n"
      "s_lshl_b32 s93, s77, 1\n"
      "s_and_b32 s95, s93, 3\n"
      "s_lshr_b32 s97, s82, s95\n"
      "s_lshl_b32 s62, s95, 14\n"
      "s_lshr_b32 s95, s93, 2\n"
      "s_xor_b32 s97, s97, s95\n"
      "s_and_b32 s97, s97, 1\n"
      "s_lshl_b32 s97, s97, 13\n"
      "s_add_u32 s62, s62, s97\n"
      "s_add_u32 s93, s93, 1\n"
      "s_and_b32 s95, s93, 3\n"
      "s_lshr_b32 s97, s82, s95\n"
      "s_lshl_b32 s63, s95, 14\n"
      "s_lshr_b32 s95, s93, 2\n"
      "s_xor_b32 s97, s97, s95\n"
      "s_and_b32 s97, s97, 1\n"
      "s_lshl_b32 s97, s97, 13\n"
      "s_add_u32 s63, s63, s97\n"
      "s_and_b32 s95, s90, 1\n"
      "s_lshl_b32 s65, s95, 16\n"
      "s_lshl_b32 s95, s95, 1\n"
      "s_lshr_b32 s97, s77, 1\n"
      "s_add_u32 s95, s95, s97\n"
      "s_lshl_b32 s64, s95, 16\n"
      "s_add_u32 s64, s64, s96\n"
      "s_and_b32 s97, s77, 1\n"
      "s_lshl_b32 s97, s97, 11\n"
      "s_add_u32 s64, s64, s97\n"
      "s_lshl_b32 s97, s89, 6\n"
      "s_add_u32 s64, s64, s97\n"
      "s_add_u32 s64, s64, 983040\n"
      "s_add_u32 s65, s65, s96\n"
      "s_add_u32 s65, s65, s97\n"
      "s_add_u32 s65, s65, s97\n"
      "s_add_u32 s65, s65, 464896\n"
      "s_lshr_b32 s95, s79, 3\n"
      "s_lshl_b32 s72, s95, 16\n"
      "s_add_u32 s72, s72, s83\n"
      "s_bfe_u32 s97, s79, 0x10002\n"
      "s_lshl_b32 s73, s97, 3\n"
      "s_lshl_b32 s97, s97, 12\n"
      "s_add_u32 s72, s72, s97\n"
      "s_lshl_b32 s97, s77, 10\n"
      "s_add_u32 s72, s72, s97\n"
      "s_add_u32 s72, s72, 983040\n"
      "s_lshr_b32 s95, s95, 1\n"
      "s_lshl_b32 s95, s95, 16\n"
      "s_add_u32 s73, s73, s95\n"
      "s_add_u32 s73, s73, s83\n"
      "s_add_u32 s73, s73, 464896\n"
      "s_cbranch_vccnz L_go71\n"
      "s_waitcnt vmcnt(8)\n"
      "s_cmpk_ge_i32 s31, 1142\n"
      "s_cbranch_scc1 L_go71\n"
      "s_waitcnt vmcnt(7)\n"
      "L_go71:\n"
      "s_add_u32 m0, s92, 25024\n"
      "s_xor_b32 s85, s86, 77824\n"
      "buffer_load_dwordx4 v51, s[36:39], s85 offen nt lds\n"
      "s_waitcnt vmcnt(11)\n"
      "L_pace72:\n"
      "s_memrealtime s[30:31]\n"
      "s_waitcnt lgkmcnt(0)\n"
      "s_sub_u32 s31, s30, s4\n"
      "s_cmpk_lt_i32 s31, 1129\n"
      "s_cbranch_scc1 L_pace72\n"
      "s_cmpk_ge_i32 s31, 1229\n"
      "s_cselect_b64 vcc, -1, 0\n"
      "s_cbranch_vccnz L_go72\n"
      "s_waitcnt vmcnt(8)\n"
      "s_cmpk_ge_i32 s31, 1169\n"
      "s_cbranch_scc1 L_go72\n"
      "s_waitcnt vmcnt(7)\n"
      "L_go72:\n"
      "s_add_u32 m0, s92, 26112\n"
      "s_xor_b32 s85, s86, 32768\n"
      "buffer_load_dwordx4 v51, s[36:39], s85 offen nt lds\n"
      "s_cmp_lg_u32 s77, 1\n"
      "s_cbranch_scc1 L_w1_1_3\n"
      "s_mov_b32 m0, 146432\n"
      "s_mov_b32 s85, 616448\n"
      "buffer_load_dwordx4 v47, s[56:59], s85 offen   lds\n"
      "L_w1_1_3:\n"
      "s_waitcnt vmcnt(11)\n"
      "s_lshr_b32 s93, s79, 4\n"
      "s_lshl_b32 s93, s93, 16\n"
      "s_add_u32 s93, s93, s83\n"
      "s_add_u32 s93, s93, 595968\n"
      "s_lshr_b32 s94, s79, 3\n"
      "s_lshl_b32 s94, s94, 2\n"
      "s_add_u32 s94, s94, s77\n"
      "s_lshr_b32 s94, s94, 1\n"
      "s_and_b32 s35, s94, 3\n"
      "s_lshl_b32 s35, s35, 9\n"
      "s_add_u32 s35, s35, s93\n"
      "s_lshl_b32 s34, s94, 10\n"
      "s_add_u32 s34, s34, s83\n"
      "s_lshr_b32 s94, s79, 2\n"
      "s_and_b32 s95, s79, 3\n"
      "s_lshl_b32 s94, s94, 5\n"
      "s_add_u32 s94, s94, s95\n"
      "s_add_u32 s94, s94, s95\n"
      "s_lshl_b32 s33, s90, 7\n"
      "s_add_u32 s33, s33, s93\n"
      "s_cmp_eq_u32 s77, 1\n"
      "s_cselect_b32 s97, 0, 0x80000000\n"
      "s_or_b32 s33, s33, s97\n"
      "s_and_b32 s68, s78, 3\n"
      "s_lshl_b32 s68, s68, 5\n"
      "s_add_u32 s68, s68, s79\n"
      "s_lshl_b32 s68, s68, 4\n"
      "s_add_u32 s68, s68, s84\n"
      "s_add_u32 s68, s68, 229376\n"
      "s_or_b32 s68, s68, s97\n"
      "s_and_b32 s5, s77, 1\n"
      "s_lshl_b32 s5, s5, 10\n"
      "s_lshr_b32 s97, s77, 1\n"
      "s_lshl_b32 s97, s97, 13\n"
      "s_add_u32 s5, s5, s97\n"
      "s_add_u32 s5, s5, 229376\n"
      "s_lshl_b32 s32, s94, 5\n"
      "s_add_u32 s32, s32, s83\n"
      "s_lshl_b32 s93, s77, 8\n"
      "s_add_u32 s32, s32, s93\n"
      "s_add_u32 s93, s93, s84\n"
      "s_and_b32 s94, s79, 1\n"
      "s_lshl_b32 s48, s94, 10\n"
      "s_add_u32 s48, s48, s93\n"
      "s_add_u32 s48, s48, 1245184\n"
      "s_lshl_b32 s94, s94, 16\n"
      "s_add_u32 s60, s94, 464256\n"
      "s_lshl_b32 s51, s88, 6\n"
      "s_add_u32 s51, s51, s94\n"
      "s_add_u32 s51, s51, 458752\n"
      "s_lshr_b32 s95, s78, 1\n"
      "s_lshl_b32 s95, s95, 14\n"
      "s_and_b32 s97, s78, 1\n"
      "s_lshl_b32 s97, s97, 12\n"
      "s_add_u32 s95, s95, s97\n"
      "s_add_u32 s95, s95, 65536\n"
      "s_add_u32 s50, s95, s94\n"
      "s_add_u32 s50, s50, s93\n"
      "s_xor_b32 s50, s50, 0\n"
      "s_and_b32 s93, s77, 1\n"
      "s_lshl_b32 s49, s79, 1\n"
      "s_add_u32 s49, s49, s93\n"
      "s_lshl_b32 s49, s49, 6\n"
      "s_add_u32 s49, s49, s95\n"
      "s_lshr_b32 s94, s77, 1\n"
      "s_lshl_b32 s94, s94, 16\n"
      "s_add_u32 s49, s49, s94\n"
      "s_mul_i32 s61, s93, 3072\n"
      "s_and_b32 s93, s89, 1\n"
      "s_lshl_b32 s93, s93, 1\n"
      "s_lshr_b32 s94, s77, 1\n"
      "s_add_u32 s93, s93, s94\n"
      "s_lshl_b32 s93, s93, 16\n"
      "s_add_u32 s61, s61, s93\n"
      "s_add_u32 s61, s61, s83\n"
      "s_add_u32 s61, s61, 458752\n"
      "s_cbranch_vccnz L_go73\n"
      "s_waitcnt vmcnt(8)\n"
      "s_cmpk_ge_i32 s31, 1169\n"
      "s_cbranch_scc1 L_go73\n"
      "s_waitcnt vmcnt(7)\n"
      "L_go73:\n"
      "s_add_u32 m0, s92, 27200\n"
      "s_xor_b32 s85, s86, 36864\n"
      "buffer_load_dwordx4 v51, s[36:39], s85 offen nt lds\n"
      "s_waitcnt vmcnt(11)\n"
      "L_pace74:\n"
      "s_memrealtime s[30:31]\n"
      "s_waitcnt lgkmcnt(0)\n"
      "s_sub_u32 s31, s30, s4\n"
      "s_cmpk_lt_i32 s31, 1157\n"
      "s_cbranch_scc1 L_pace74\n"
      "s_cmpk_ge_i32 s31, 1257\n"
      "s_cselect_b64 vcc, -1, 0\n"
      "s_cbranch_vccnz L_go74\n"
      "s_waitcnt vmcnt(8)\n"
      "s_cmpk_ge_i32 s31, 1197\n"
      "s_cbranch_scc1 L_go74\n"
      "s_waitcnt vmcnt(4)\n"
      "L_go74:\n"
      "s_add_u32 m0, s92, 28288\n"
      "s_xor_b32 s85, s86, 40960\n"
      "buffer_load_dwordx4 v51, s[36:39], s85 offen nt lds\n"
      "s_waitcnt vmcnt(11)\n"
      "s_cbranch_vccnz L_go75\n"
      "s_waitcnt vmcnt(5)\n"
      "s_cmpk_ge_i32 s31, 1197\n"
      "s_cbranch_scc1 L_go75\n"
      "s_waitcnt vmcnt(4)\n"
      "L_go75:\n"
      "s_add_u32 m0, s92, 29376\n"
      "s_xor_b32 s85, s86, 45056\n"
      "buffer_load_dwordx4 v51, s[36:39], s85 offen nt lds\n"
      "s_waitcnt vmcnt(11)\n"
      "ds_read_b64_tr_b16 a[192:193], v3 offset:17408\n"
      "ds_read_b64_tr_b16 a[200:201], v4 offset:17408\n"
      "ds_read_b64_tr_b16 a[208:209], v3 offset:17472\n"
      "ds_read_b64_tr_b16 a[216:217], v4 offset:17472\n"
      "ds_read_b64_tr_b16 a[224:225], v3 offset:17536\n"
      "ds_read_b64_tr_b16 a[232:233], v4 offset:17536\n"
      "ds_read_b64_tr_b16 a[240:241], v3 offset:17600\n"
      "ds_read_b64_tr_b16 a[248:249], v4 offset:17600\n"
      "L_pace76:\n"
      "s_memrealtime s[30:31]\n"
      "s_waitcnt lgkmcnt(0)\n"
      "s_sub_u32 s31, s30, s4\n"
      "s_cmpk_lt_i32 s31, 1185\n"
      "s_cbranch_scc1 L_pace76\n"
      "s_cmpk_ge_i32 s31, 1285\n"
      "s_cselect_b64 vcc, -1, 0\n"
      "s_cbranch_vccnz L_go76\n"
      "s_waitcnt vmcnt(5)\n"
      "s_cmpk_ge_i32 s31, 1225\n"
      "s_cbranch_scc1 L_go76\n"
      "s_waitcnt vmcnt(4)\n"
      "L_go76:\n"
      "s_add_u32 m0, s92, 30464\n"
      "s_xor_b32 s85, s86, 98304\n"
      "buffer_load_dwordx4 v51, s[36:39], s85 offen nt lds\n"
      "s_cmp_lg_u32 s77, 2\n"
      "s_cbranch_scc1 L_w1_2_3\n"
      "s_mov_b32 m0, 150528\n"
      "s_mov_b32 s85, 968704\n"
      "buffer_load_dwordx4 v47, s[56:59], s85 offen   lds\n"
      "L_w1_2_3:\n"
      "s_waitcnt vmcnt(11)\n"
      "s_cbranch_vccnz L_go77\n"
      "s_waitcnt vmcnt(5)\n"
      "s_cmpk_ge_i32 s31, 1225\n"
      "s_cbranch_scc1 L_go77\n"
      "s_waitcnt vmcnt(4)\n"
      "L_go77:\n"
      "s_add_u32 m0, s92, 31552\n"
      "s_xor_b32 s85, s86, 102400\n"
      "buffer_load_dwordx4 v51, s[36:39], s85 offen nt lds\n"
      "s_waitcnt vmcnt(8)\n"
      "s_waitcnt lgkmcnt(0)\n"
      "v_mfma_f32_16x16x32_bf16 v[40:43], v[16:19], v[36:39], 0\n"
      "s_nop 3\n"
      "v_mov_b32 v0, v0\n"
      "s_nop 2\n"
      "v_permlane32_swap_b32 v40, v42\n"
      "v_permlane32_swap_b32 v41, v43\n"
      "v_add_f32 v40, v40, v42\n"
      "v_add_f32 v41, v41, v43\n"
      "s_nop 1\n"
      "v_permlane16_swap_b32 v40, v41\n"
      "v_add_f32 v40, v40, v41\n"
      "v_mul_f32 v6, 0xbb000000, v40\n"
      "v_add_f32_dpp v12, v13, v13 row_ror:4 row_mask:0xf bank_mask:0xf\n"
      "v_mfma_f32_16x16x32_bf16 v[40:43], v[32:35], v[36:39], 0\n"
      "s_nop 0\n"
      "v_add_f32_dpp v12, v12, v12 row_ror:8 row_mask:0xf bank_mask:0xf\n"
      "v_mov_b32 v8, v12\n"
      "s_nop 1\n"
      "v_permlane16_swap_b32 v12, v8\n"
      "v_add_f32 v12, v12, v8\n"
      "v_mov_b32 v8, v12\n"
      "v_permlane32_swap_b32 v40, v42\n"
      "v_permlane32_swap_b32 v41, v43\n"
      "v_add_f32 v40, v40, v42\n"
      "v_add_f32 v41, v41, v43\n"
      "v_permlane32_swap_b32 v12, v8\n"
      "v_add_f32 v12, v12, v8\n"
      "v_permlane16_swap_b32 v40, v41\n"
      "v_add_f32 v40, v40, v41\n"
      "v_mul_f32_dpp v8, v12, v6 quad_perm:[0,0,0,0] row_mask:0xf bank_mask:0xf\n"
      "v_add_f32 v40, v40, v8\n"
      "v_cvt_pk_bf16_f32 v40, v40, v40\n"
      "ds_write_b16 v14, v40\n"
      "v_mov_b32 v13, 0x3b000000\n"
      "v_mov_b32 v15, 0x3727c5ac\n"
      "s_nop 0\n"
      "v_mul_f32_dpp v14, v12, v13 quad_perm:[0,0,0,0] row_mask:0xf bank_mask:0xf\n"
      "v_fma_f32 v14, -v14, v14, v15\n"
      "s_nop 1\n"
      "v_fmac_f32_dpp v14, v12, v13 quad_perm:[1,1,1,1] row_mask:0xf bank_mask:0xf\n"
      "v_max_f32 v14, v14, v15\n"
      "v_rsq_f32 v14, v14\n"
      "s_nop 0\n"
      "v_mul_f32 v8, 0x3e0293ee, v14\n"
      "L_pace78:\n"
      "s_memrealtime s[30:31]\n"
      "s_waitcnt lgkmcnt(0)\n"
      "s_sub_u32 s31, s30, s4\n"
      "s_cmpk_lt_i32 s31, 1212\n"
      "s_cbranch_scc1 L_pace78\n"
      "s_cmpk_ge_i32 s31, 1312\n"
      "s_cselect_b64 vcc, -1, 0\n"
      "s_cbranch_vccnz L_go78\n"
      "s_waitcnt vmcnt(5)\n"
      "s_cmpk_ge_i32 s31, 1252\n"
      "s_cbranch_scc1 L_go78\n"
      "s_waitcnt vmcnt(4)\n"
      "L_go78:\n"
      "s_add_u32 m0, s92, 32640\n"
      "s_xor_b32 s85, s86, 106496\n"
      "buffer_load_dwordx4 v51, s[36:39], s85 offen nt lds\n"
      "s_waitcnt vmcnt(8)\n"
      "s_cbranch_vccnz L_go79\n"
      "s_waitcnt vmcnt(5)\n"
      "s_cmpk_ge_i32 s31, 1252\n"
      "s_cbranch_scc1 L_go79\n"
      "s_waitcnt vmcnt(4)\n"
      "L_go79:\n"
      "s_add_u32 m0, s92, 33728\n"
      "s_xor_b32 s85, s86, 110592\n"
      "buffer_load_dwordx4 v51, s[36:39], s85 offen nt lds\n"
      "s_waitcnt vmcnt(8)\n"
      "ds_read_b64_tr_b16 a[196:197], v3 offset:21760\n"
      "ds_read_b64_tr_b16 a[204:205], v4 offset:21760\n"
      "ds_read_b64_tr_b16 a[212:213], v3 offset:21824\n"
      "ds_read_b64_tr_b16 a[220:221], v4 offset:21824\n"
      "ds_read_b64_tr_b16 a[228:229], v3 offset:21888\n"
      "ds_read_b64_tr_b16 a[236:237], v4 offset:21888\n"
      "ds_read_b64_tr_b16 a[244:245], v3 offset:21952\n"
      "ds_read_b64_tr_b16 a[252:253], v4 offset:21952\n"
      "L_pace80:\n"
      "s_memrealtime s[30:31]\n"
      "s_waitcnt lgkmcnt(0)\n"
      "s_sub_u32 s31, s30, s4\n"
      "s_cmpk_lt_i32 s31, 1240\n"
      "s_cbranch_scc1 L_pace80\n"
      "s_cmpk_ge_i32 s31, 1340\n"
      "s_cselect_b64 vcc, -1, 0\n"
      "s_cbranch_vccnz L_go80\n"
      "s_waitcnt vmcnt(5)\n"
      "s_cmpk_ge_i32 s31, 1280\n"
      "s_cbranch_scc1 L_go80\n"
      "s_waitcnt vmcnt(4)\n"
      "L_go80:\n"
      "s_add_u32 m0, s92, 17408\n"
      "s_xor_b32 s85, s86, 131072\n"
      "buffer_load_dwordx4 v51, s[36:39], s85 offen nt lds\n"
      "s_cmp_lg_u32 s77, 3\n"
      "s_cbranch_scc1 L_w1_3_3\n"
      "s_mov_b32 m0, 154624\n"
      "s_mov_b32 s85, 1320960\n"
      "buffer_load_dwordx4 v47, s[56:59], s85 offen   lds\n"
      "L_w1_3_3:\n"
      "s_waitcnt vmcnt(8)\n"
      "s_cbranch_vccnz L_go81\n"
      "s_waitcnt vmcnt(5)\n"
      "s_cmpk_ge_i32 s31, 1280\n"
      "s_cbranch_scc1 L_go81\n"
      "s_waitcnt vmcnt(4)\n"
      "L_go81:\n"
      "s_add_u32 m0, s92, 18496\n"
      "s_xor_b32 s85, s86, 135168\n"
      "buffer_load_dwordx4 v51, s[36:39], s85 offen nt lds\n"
      "s_waitcnt vmcnt(8)\n"
      "L_pace82:\n"
      "s_memrealtime s[30:31]\n"
      "s_waitcnt lgkmcnt(0)\n"
      "s_sub_u32 s31, s30, s4\n"
      "s_cmpk_lt_i32 s31, 1268\n"
      "s_cbranch_scc1 L_pace82\n"
      "s_cmpk_ge_i32 s31, 1368\n"
      "s_cselect_b64 vcc, -1, 0\n"
      "s_cbranch_vccnz L_go82\n"
      "s_waitcnt vmcnt(5)\n"
      "s_cmpk_ge_i32 s31, 1308\n"
      "s_cbranch_scc1 L_go82\n"
      "s_waitcnt vmcnt(4)\n"
      "L_go82:\n"
      "s_add_u32 m0, s92, 19584\n"
      "s_xor_b32 s85, s86, 139264\n"
      "buffer_load_dwordx4 v51, s[36:39], s85 offen nt lds\n"
      "s_waitcnt vmcnt(8)\n"
      "s_cbranch_vccnz L_go83\n"
      "s_waitcnt vmcnt(5)\n"
      "s_cmpk_ge_i32 s31, 1308\n"
      "s_cbranch_scc1 L_go83\n"
      "s_waitcnt vmcnt(4)\n"
      "L_go83:\n"
      "s_add_u32 m0, s92, 20672\n"
      "s_xor_b32 s85, s86, 143360\n"
      "buffer_load_dwordx4 v51, s[36:39], s85 offen nt lds\n"
      "s_waitcnt vmcnt(8)\n"
      "ds_read_b64_tr_b16 a[194:195], v3 offset:26112\n"
      "ds_read_b64_tr_b16 a[202:203], v4 offset:26112\n"
      "ds_read_b64_tr_b16 a[210:211], v3 offset:26176\n"
      "ds_read_b64_tr_b16 a[218:219], v4 offset:26176\n"
      "ds_read_b64_tr_b16 a[226:227], v3 offset:26240\n"
      "ds_read_b64_tr_b16 a[234:235], v4 offset:26240\n"
      "ds_read_b64_tr_b16 a[242:243], v3 offset:26304\n"
      "ds_read_b64_tr_b16 a[250:251], v4 offset:26304\n"
      "L_pace84:\n"
      "s_memrealtime s[30:31]\n"
      "s_waitcnt lgkmcnt(0)\n"
      "s_sub_u32 s31, s30, s4\n"
      "s_cmpk_lt_i32 s31, 1295\n"
      "s_cbranch_scc1 L_pace84\n"
      "s_cmpk_ge_i32 s31, 1395\n"
      "s_cselect_b64 vcc, -1, 0\n"
      "s_cbranch_vccnz L_go84\n"
      "s_waitcnt vmcnt(5)\n"
      "s_cmpk_ge_i32 s31, 1335\n"
      "s_cbranch_scc1 L_go84\n"
      "s_waitcnt vmcnt(4)\n"
      "L_go84:\n"
      "s_add_u32 m0, s92, 21760\n"
      "s_xor_b32 s85, s86, 196608\n"
      "buffer_load_dwordx4 v51, s[36:39], s85 offen nt lds\n"
      "s_waitcnt vmcnt(8)\n"
      "s_cbranch_vccnz L_go85\n"
      "s_waitcnt vmcnt(5)\n"
      "s_cmpk_ge_i32 s31, 1335\n"
      "s_cbranch_scc1 L_go85\n"
      "s_waitcnt vmcnt(4)\n"
      "L_go85:\n"
      "s_add_u32 m0, s92, 22848\n"
      "s_xor_b32 s85, s86, 200704\n"
      "buffer_load_dwordx4 v51, s[36:39], s85 offen nt lds\n"
      "s_waitcnt vmcnt(8)\n"
      "L_pace86:\n"
      "s_memrealtime s[30:31]\n"
      "s_waitcnt lgkmcnt(0)\n"
      "s_sub_u32 s31, s30, s4\n"
      "s_cmpk_lt_i32 s31, 1322\n"
      "s_cbranch_scc1 L_pace86\n"
      "s_cmpk_ge_i32 s31, 1422\n"
      "s_cselect_b64 vcc, -1, 0\n"
      "s_cbranch_vccnz L_go86\n"
      "s_waitcnt vmcnt(5)\n"
      "s_cmpk_ge_i32 s31, 1362\n"
      "s_cbranch_scc1 L_go86\n"
      "s_waitcnt vmcnt(4)\n"
      "L_go86:\n"
      "s_add_u32 m0, s92, 23936\n"
      "s_xor_b32 s85, s86, 204800\n"
      "buffer_load_dwordx4 v51, s[36:39], s85 offen nt lds\n"
      "s_waitcnt vmcnt(8)\n"
      "s_cbranch_vccnz L_go87\n"
      "s_waitcnt vmcnt(5)\n"
      "s_cmpk_ge_i32 s31, 1362\n"
      "s_cbranch_scc1 L_go87\n"
      "s_waitcnt vmcnt(4)\n"
      "L_go87:\n"
      "s_add_u32 m0, s92, 25024\n"
      "s_xor_b32 s85, s86, 208896\n"
      "buffer_load_dwordx4 v51, s[36:39], s85 offen nt lds\n"
      "s_waitcnt vmcnt(8)\n"
      "ds_read_b64_tr_b16 a[198:199], v3 offset:30464\n"
      "ds_read_b64_tr_b16 a[206:207], v4 offset:30464\n"
      "ds_read_b64_tr_b16 a[214:215], v3 offset:30528\n"
      "ds_read_b64_tr_b16 a[222:223], v4 offset:30528\n"
      "ds_read_b64_tr_b16 a[230:231], v3 offset:30592\n"
      "ds_read_b64_tr_b16 a[238:239], v4 offset:30592\n"
      "ds_read_b64_tr_b16 a[246:247], v3 offset:30656\n"
      "ds_read_b64_tr_b16 a[254:255], v4 offset:30656\n"
      "L_pace88:\n"
      "s_memrealtime s[30:31]\n"
      "s_waitcnt lgkmcnt(0)\n"
      "s_sub_u32 s31, s30, s4\n"
      "s_cmpk_lt_i32 s31, 1349\n"
      "s_cbranch_scc1 L_pace88\n"
      "s_cmpk_ge_i32 s31, 1449\n"
      "s_cselect_b64 vcc, -1, 0\n"
      "s_cbranch_vccnz L_go88\n"
      "s_waitcnt vmcnt(5)\n"
      "s_cmpk_ge_i32 s31, 1389\n"
      "s_cbranch_scc1 L_go88\n"
      "s_waitcnt vmcnt(4)\n"
      "L_go88:\n"
      "s_add_u32 m0, s92, 26112\n"
      "s_xor_b32 s85, s86, 163840\n"
      "buffer_load_dwordx4 v51, s[36:39], s85 offen nt lds\n"
      "s_waitcnt vmcnt(8)\n"
      "s_cbranch_vccnz L_go89\n"
      "s_waitcnt vmcnt(5)\n"
      "s_cmpk_ge_i32 s31, 1389\n"
      "s_cbranch_scc1 L_go89\n"
      "s_waitcnt vmcnt(4)\n"
      "L_go89:\n"
      "s_add_u32 m0, s92, 27200\n"
      "s_xor_b32 s85, s86, 167936\n"
      "buffer_load_dwordx4 v51, s[36:39], s85 offen nt lds\n"
      "s_waitcnt vmcnt(8)\n"
      "L_pace90:\n"
      "s_memrealtime s[30:31]\n"
      "s_waitcnt lgkmcnt(0)\n"
      "s_sub_u32 s31, s30, s4\n"
      "s_cmpk_lt_i32 s31, 1376\n"
      "s_cbranch_scc1 L_pace90\n"
      "s_cmpk_ge_i32 s31, 1476\n"
      "s_cselect_b64 vcc, -1, 0\n"
      "s_cbranch_vccnz L_go90\n"
      "s_waitcnt vmcnt(5)\n"
      "s_cmpk_ge_i32 s31, 1416\n"
      "s_cbranch_scc1 L_go90\n"
      "s_waitcnt vmcnt(4)\n"
      "L_go90:\n"
      "s_add_u32 m0, s92, 28288\n"
      "s_xor_b32 s85, s86, 172032\n"
      "buffer_load_dwordx4 v51, s[36:39], s85 offen nt lds\n"
      "s_waitcnt vmcnt(8)\n"
      "s_lshl_b32 s95, s77, 12\n"
      "s_add_u32 s95, s95, 139264\n"
      "v_lshl_add_u32 v26, v7, 3, s95\n"
      "v_and_b32 v27, 0x30, v0\n"
      "v_lshl_add_u32 v26, v27, 4, v26\n"
      "ds_read_b64_tr_b16 v[96:97], v26 offset:0\n"
      "ds_read_b64_tr_b16 v[98:99], v26 offset:128\n"
      "ds_read_b64_tr_b16 v[100:101], v26 offset:1024\n"
      "ds_read_b64_tr_b16 v[102:103], v26 offset:1152\n"
      "ds_read_b64_tr_b16 v[104:105], v26 offset:2048\n"
      "ds_read_b64_tr_b16 v[106:107], v26 offset:2176\n"
      "ds_read_b64_tr_b16 v[108:109], v26 offset:3072\n"
      "ds_read_b64_tr_b16 v[110:111], v26 offset:3200\n"
      "v_mov_b32 v28, 0x3f803f80\n"
      "v_mov_b32 v29, 0x3f803f80\n"
      "v_mov_b32 v30, 0x3f803f80\n"
      "v_mov_b32 v31, 0x3f803f80\n"
      "v_mov_b64 v[52:53], 0\n"
      "s_waitcnt lgkmcnt(0)\n"
      "s_nop 0\n"
      "v_smfmac_f32_16x16x64_bf16 v[52:55], v[28:31], v[96:103], v1\n"
      "v_smfmac_f32_16x16x64_bf16 v[52:55], v[28:31], v[104:111], v1\n"
      "s_lshl_b32 s95, s77, 2\n"
      "s_add_u32 s95, s95, 163072\n"
      "v_lshl_add_u32 v26, v7, 4, s95\n"
      "s_nop 1\n"
      "v_mov_b32 v0, v0\n"
      "s_nop 1\n"
      "v_add_f32 v27, v52, v53\n"
      "ds_write_b32 v26, v27\n"
      "s_cmp_lg_u32 s77, 0\n"
      "s_cbranch_scc1 L_nt_d\n"
      "s_call_b64 s[66:67], L_stub_d\n"
      "L_nt_d:\n"
      "s_cmp_lg_u32 s77, 1\n"
      "s_cbranch_scc1 L_nt_a\n"
      "s_call_b64 s[66:67], L_stub_a\n"
      "L_nt_a:\n"
      "s_cmp_lg_u32 s77, 2\n"
      "s_cbranch_scc1 L_nt_b\n"
      "s_call_b64 s[66:67], L_stub_b\n"
      "L_nt_b:\n"
      "s_cmp_lg_u32 s77, 3\n"
      "s_cbranch_scc1 L_nt_g\n"
      "s_call_b64 s[66:67], L_stub_g\n"
      "L_nt_g:\n"
      "s_cbranch_vccnz L_go91\n"
      "s_waitcnt vmcnt(5)\n"
      "s_cmpk_ge_i32 s31, 1416\n"
      "s_cbranch_scc1 L_go91\n"
      "s_waitcnt vmcnt(4)\n"
      "L_go91:\n"
      "s_add_u32 m0, s92, 29376\n"
      "s_xor_b32 s85, s86, 176128\n"
      "buffer_load_dwordx4 v51, s[36:39], s85 offen nt lds\n"
      "s_waitcnt vmcnt(8)\n"
      "ds_read_b64_tr_b16 v[128:129], v3 offset:17408\n"
      "ds_read_b64_tr_b16 v[136:137], v4 offset:17408\n"
      "ds_read_b64_tr_b16 v[144:145], v3 offset:17472\n"
      "ds_read_b64_tr_b16 v[152:153], v4 offset:17472\n"
      "ds_read_b64_tr_b16 v[160:161], v3 offset:17536\n"
      "ds_read_b64_tr_b16 v[168:169], v4 offset:17536\n"
      "ds_read_b64_tr_b16 v[176:177], v3 offset:17600\n"
      "ds_read_b64_tr_b16 v[184:185], v4 offset:17600\n"
      "L_pace92:\n"
      "s_memrealtime s[30:31]\n"
      "s_waitcnt lgkmcnt(0)\n"
      "s_sub_u32 s31, s30, s4\n"
      "s_cmpk_lt_i32 s31, 1404\n"
      "s_cbranch_scc1 L_pace92\n"
      "s_cmpk_ge_i32 s31, 1504\n"
      "s_cselect_b64 vcc, -1, 0\n"
      "s_cbranch_vccnz L_go92\n"
      "s_waitcnt vmcnt(5)\n"
      "s_cmpk_ge_i32 s31, 1444\n"
      "s_cbranch_scc1 L_go92\n"
      "s_waitcnt vmcnt(4)\n"
      "L_go92:\n"
      "s_add_u32 m0, s92, 30464\n"
      "s_xor_b32 s85, s86, 229376\n"
      "buffer_load_dwordx4 v51, s[36:39], s85 offen nt lds\n"
      "s_branch L_over_k93\n"
      "L_pend_k93:\n"
      ".p2align 12\n"
      "L_stub_k93:\n"
      "s_setpc_b64 s[66:67]\n"
      "L_over_k93:\n"
      "s_waitcnt vmcnt(8)\n"
      "s_cmp_lg_u32 s77, 0\n"
      "s_cbranch_scc1 L_nt_f\n"
      "s_call_b64 s[66:67], L_stub_f\n"
      "L_nt_f:\n"
      "s_cmp_lg_u32 s77, 1\n"
      "s_cbranch_scc1 L_nt_t\n"
      "s_call_b64 s[66:67], L_stub_t\n"
      "L_nt_t:\n"
      "s_cmp_lg_u32 s77, 2\n"
      "s_cbranch_scc1 L_nt_r\n"
      "s_call_b64 s[66:67], L_stub_r\n"
      "L_nt_r:\n"
      "s_cmp_lg_u32 s77, 3\n"
      "s_cbranch_scc1 L_nt_e\n"
      "s_call_b64 s[66:67], L_stub_e\n"
      "L_nt_e:\n"
      "s_cbranch_vccnz L_go93\n"
      "s_waitcnt vmcnt(5)\n"
      "s_cmpk_ge_i32 s31, 1444\n"
      "s_cbranch_scc1 L_go93\n"
      "s_waitcnt vmcnt(4)\n"
      "L_go93:\n"
      "s_add_u32 m0, s92, 31552\n"
      "s_xor_b32 s85, s86, 233472\n"
      "buffer_load_dwordx4 v51, s[36:39], s85 offen nt lds\n"
      "s_waitcnt vmcnt(8)\n"
      "L_pace94:\n"
      "s_memrealtime s[30:31]\n"
      "s_waitcnt lgkmcnt(0)\n"
      "s_sub_u32 s31, s30, s4\n"
      "s_cmpk_lt_i32 s31, 1431\n"
      "s_cbranch_scc1 L_pace94\n"
      "s_cmpk_ge_i32 s31, 1531\n"
      "s_cselect_b64 vcc, -1, 0\n"
      "s_cbranch_vccnz L_go94\n"
      "s_waitcnt vmcnt(5)\n"
      "s_cmpk_ge_i32 s31, 1471\n"
      "s_cbranch_scc1 L_go94\n"
      "s_waitcnt vmcnt(4)\n"
      "L_go94:\n"
      "s_add_u32 m0, s92, 32640\n"
      "s_xor_b32 s85, s86, 237568\n"
      "buffer_load_dwordx4 v51, s[36:39], s85 offen nt lds\n"
      "s_waitcnt vmcnt(8)\n"
      "s_cmp_lg_u32 s77, 0\n"
      "s_cbranch_scc1 L_nt_p\n"
      "s_call_b64 s[66:67], L_stub_p\n"
      "L_nt_p:\n"
      "s_cmp_lg_u32 s77, 1\n"
      "s_cbranch_scc1 L_nt_h\n"
      "s_call_b64 s[66:67], L_stub_h\n"
      "L_nt_h:\n"
      "s_cmp_lg_u32 s77, 2\n"
      "s_cbranch_scc1 L_nt_i\n"
      "s_call_b64 s[66:67], L_stub_i\n"
      "L_nt_i:\n"
      "s_cbranch_vccnz L_go95\n"
      "s_waitcnt vmcnt(5)\n"
      "s_cmpk_ge_i32 s31, 1471\n"
      "s_cbranch_scc1 L_go95\n"
      "s_waitcnt vmcnt(4)\n"
      "L_go95:\n"
      "s_add_u32 m0, s92, 33728\n"
      "s_xor_b32 s85, s86, 241664\n"
      "buffer_load_dwordx4 v51, s[36:39], s85 offen nt lds\n"
      "s_waitcnt vmcnt(8)\n"
      "ds_read_b64_tr_b16 v[132:133], v3 offset:21760\n"
      "ds_read_b64_tr_b16 v[140:141], v4 offset:21760\n"
      "ds_read_b64_tr_b16 v[148:149], v3 offset:21824\n"
      "ds_read_b64_tr_b16 v[156:157], v4 offset:21824\n"
      "ds_read_b64_tr_b16 v[164:165], v3 offset:21888\n"
      "ds_read_b64_tr_b16 v[172:173], v4 offset:21888\n"
      "ds_read_b64_tr_b16 v[180:181], v3 offset:21952\n"
      "ds_read_b64_tr_b16 v[188:189], v4 offset:21952\n"
      "L_pace96:\n"
      "s_memrealtime s[30:31]\n"
      "s_waitcnt lgkmcnt(0)\n"
      "s_sub_u32 s31, s30, s4\n"
      "s_cmpk_lt_i32 s31, 1458\n"
      "s_cbranch_scc1 L_pace96\n"
      "s_cmpk_ge_i32 s31, 1558\n"
      "s_cselect_b64 vcc, -1, 0\n"
      "s_cbranch_vccnz L_go96\n"
      "s_waitcnt vmcnt(5)\n"
      "s_cmpk_ge_i32 s31, 1498\n"
      "s_cbranch_scc1 L_go96\n"
      "s_waitcnt vmcnt(4)\n"
      "L_go96:\n"
      "s_add_u32 m0, s92, 17408\n"
      "s_xor_b32 s85, s86, 1024\n"
      "buffer_load_dwordx4 v51, s[36:39], s85 offen nt lds\n"
      "s_waitcnt vmcnt(8)\n"
      "s_cbranch_vccnz L_go97\n"
      "s_waitcnt vmcnt(5)\n"
      "s_cmpk_ge_i32 s31, 1498\n"
      "s_cbranch_scc1 L_go97\n"
      "s_waitcnt vmcnt(4)\n"
      "L_go97:\n"
      "s_add_u32 m0, s92, 18496\n"
      "s_xor_b32 s85, s86, 5120\n"
      "buffer_load_dwordx4 v51, s[36:39], s85 offen nt lds\n"
      "s_waitcnt vmcnt(8)\n"
      "L_pace98:\n"
      "s_memrealtime s[30:31]\n"
      "s_waitcnt lgkmcnt(0)\n"
      "s_sub_u32 s31, s30, s4\n"
      "s_cmpk_lt_i32 s31, 1485\n"
      "s_cbranch_scc1 L_pace98\n"
      "s_cmpk_ge_i32 s31, 1585\n"
      "s_cselect_b64 vcc, -1, 0\n"
      "s_cbranch_vccnz L_go98\n"
      "s_waitcnt vmcnt(5)\n"
      "s_cmpk_ge_i32 s31, 1525\n"
      "s_cbranch_scc1 L_go98\n"
      "s_waitcnt vmcnt(4)\n"
      "L_go98:\n"
      "s_add_u32 m0, s92, 19584\n"
      "s_xor_b32 s85, s86, 9216\n"
      "buffer_load_dwordx4 v51, s[36:39], s85 offen nt lds\n"
      "s_waitcnt vmcnt(8)\n"
      "ds_read_b128 v[16:19], v2 offset:0\n"
      "ds_read_b128 v[20:23], v2 offset:64\n"
      "v_mov_b64 v[48:49], 0\n"
      "ds_read_b128 v[24:27], v2 offset:4352\n"
      "ds_read_b128 v[28:31], v2 offset:4416\n"
      "v_mov_b64 v[52:53], 0\n"
      "ds_read_b128 v[64:67], v2 offset:8704\n"
      "ds_read_b128 v[68:71], v2 offset:8768\n"
      "v_mov_b64 v[56:57], 0\n"
      "ds_read_b128 v[72:75], v2 offset:13056\n"
      "ds_read_b128 v[76:79], v2 offset:13120\n"
      "v_mov_b64 v[60:61], 0\n"
      "v_bfe_u32 v7, v0, 2, 2\n"
      "v_mul_u32_u24 v7, 320, v7\n"
      "v_sub_u32 v7, 160960, v7\n"
      "v_lshrrev_b32 v15, 4, v0\n"
      "v_lshl_add_u32 v7, v15, 5, v7\n"
      "v_and_b32 v15, 1, v0\n"
      "v_lshl_add_u32 v7, v15, 4, v7\n"
      "s_lshl_b32 s95, s77, 9\n"
      "v_and_b32 v9, 0x39, v0\n"
      "v_lshlrev_b32 v9, 3, v9\n"
      "v_and_b32 v15, 2, v0\n"
      "v_lshl_add_u32 v9, v15, 4, v9\n"
      "v_and_b32 v15, 4, v0\n"
      "v_lshl_add_u32 v9, v15, 2, v9\n"
      "s_add_u32 s95, s95, 155648\n"
      "v_add_u32 v9, s95, v9\n"
      "s_cbranch_vccnz L_go99\n"
      "s_waitcnt vmcnt(5)\n"
      "s_cmpk_ge_i32 s31, 1525\n"
      "s_cbranch_scc1 L_go99\n"
      "s_waitcnt vmcnt(4)\n"
      "L_go99:\n"
      "s_add_u32 m0, s92, 20672\n"
      "s_xor_b32 s85, s86, 13312\n"
      "buffer_load_dwordx4 v51, s[36:39], s85 offen nt lds\n"
      "s_add_u32 m0, s92, 21760\n"
      "s_xor_b32 s85, s86, 66560\n"
      "buffer_load_dwordx4 v51, s[36:39], s85 offen nt lds\n"
      "s_add_u32 m0, s92, 22848\n"
      "s_xor_b32 s85, s86, 70656\n"
      "buffer_load_dwordx4 v51, s[36:39], s85 offen nt lds\n"
      "s_add_u32 m0, s92, 23936\n"
      "s_xor_b32 s85, s86, 74752\n"
      "buffer_load_dwordx4 v51, s[36:39], s85 offen nt lds\n"
      "s_add_u32 m0, s92, 25024\n"
      "s_xor_b32 s85, s86, 78848\n"
      "buffer_load_dwordx4 v51, s[36:39], s85 offen nt lds\n"
      "s_waitcnt vmcnt(12)\n"
      "ds_read_b64_tr_b16 v[130:131], v3 offset:26112\n"
      "ds_read_b64_tr_b16 v[138:139], v4 offset:26112\n"
      "ds_read_b64_tr_b16 v[146:147], v3 offset:26176\n"
      "ds_read_b64_tr_b16 v[154:155], v4 offset:26176\n"
      "ds_read_b64_tr_b16 v[162:163], v3 offset:26240\n"
      "ds_read_b64_tr_b16 v[170:171], v4 offset:26240\n"
      "ds_read_b64_tr_b16 v[178:179], v3 offset:26304\n"
      "ds_read_b64_tr_b16 v[186:187], v4 offset:26304\n"
      "s_waitcnt lgkmcnt(0)\n"
      "s_add_u32 m0, s92, 26112\n"
      "s_xor_b32 s85, s86, 33792\n"
      "buffer_load_dwordx4 v51, s[36:39], s85 offen nt lds\n"
      "s_waitcnt vmcnt(12)\n"
      "s_add_u32 m0, s92, 27200\n"
      "s_xor_b32 s85, s86, 37888\n"
      "buffer_load_dwordx4 v51, s[36:39], s85 offen nt lds\n"
      "s_waitcnt vmcnt(12)\n"
      "s_add_u32 m0, s92, 28288\n"
      "s_xor_b32 s85, s86, 41984\n"
      "buffer_load_dwordx4 v51, s[36:39], s85 offen nt lds\n"
      "s_waitcnt vmcnt(12)\n"
      "s_add_u32 m0, s92, 29376\n"
      "s_xor_b32 s85, s86, 46080\n"
      "buffer_load_dwordx4 v51, s[36:39], s85 offen nt lds\n"
      "s_waitcnt vmcnt(12)\n"
      "ds_read_b64_tr_b16 v[134:135], v3 offset:30464\n"
      "ds_read_b64_tr_b16 v[142:143], v4 offset:30464\n"
      "ds_read_b64_tr_b16 v[150:151], v3 offset:30528\n"
      "ds_read_b64_tr_b16 v[158:159], v4 offset:30528\n"
      "ds_read_b64_tr_b16 v[166:167], v3 offset:30592\n"
      "ds_read_b64_tr_b16 v[174:175], v4 offset:30592\n"
      "ds_read_b64_tr_b16 v[182:183], v3 offset:30656\n"
      "ds_read_b64_tr_b16 v[190:191], v4 offset:30656\n"
      "v_readfirstlane_b32 s69, v51\n"
      "v_mov_b32 v50, 0x80000000\n"
      "s_waitcnt lgkmcnt(0)\n"
      "s_barrier\n"
      "s_branch L_att\n"
      "L_hook:\n"
      "v_bfe_u32 v13, v0, 4, 1\n"
      "v_lshlrev_b32 v12, 8, v13\n"
      "v_lshrrev_b32 v7, 5, v0\n"
      "v_lshl_or_b32 v12, v7, 9, v12\n"
      "v_xor_b32 v12, s69, v12\n"
      "v_lshlrev_b32 v13, 1, v13\n"
      "v_xor_b32 v13, v13, v0\n"
      "v_and_b32 v13, 15, v13\n"
      "v_lshl_or_b32 v12, v13, 4, v12\n"
      "v_lshrrev_b32 v15, 3, v0\n"
      "v_and_b32 v13, 2, v15\n"
      "v_xor_b32 v13, v13, v0\n"
      "v_and_b32 v13, 7, v13\n"
      "v_lshlrev_b32 v15, 10, v15\n"
      "v_lshl_or_b32 v15, v13, 4, v15\n"
      "s_and_b32 s95, s77, 1\n"
      "s_lshl_b32 s95, s95, 6\n"
      "v_xor_b32 v15, s95, v15\n"
      "s_lshl_b32 s85, s78, 2\n"
      "s_lshr_b32 s95, s79, 3\n"
      "s_add_u32 s85, s85, s95\n"
      "s_lshl_b32 s85, s85, 2\n"
      "s_add_u32 s85, s85, s77\n"
      "s_lshl_b32 s85, s85, 15\n"
      "s_and_b32 s95, s79, 7\n"
      "s_lshl_b32 s95, s95, 7\n"
      "s_add_u32 s85, s85, s95\n"
      "v_add_u32 v15, s85, v15\n"
      "s_waitcnt vmcnt(10)\n"
      "ds_read_b64_tr_b16 v[192:193], v3 offset:17408\n"
      "ds_read_b64_tr_b16 v[200:201], v4 offset:17408\n"
      "ds_read_b64_tr_b16 v[208:209], v3 offset:17472\n"
      "ds_read_b64_tr_b16 v[216:217], v4 offset:17472\n"
      "ds_read_b64_tr_b16 v[224:225], v3 offset:17536\n"
      "ds_read_b64_tr_b16 v[232:233], v4 offset:17536\n"
      "ds_read_b64_tr_b16 v[240:241], v3 offset:17600\n"
      "ds_read_b64_tr_b16 v[248:249], v4 offset:17600\n"
      "L_pace108:\n"
      "s_memrealtime s[30:31]\n"
      "s_waitcnt lgkmcnt(0)\n"
      "s_sub_u32 s31, s30, s4\n"
      "s_cmpk_lt_i32 s31, 1621\n"
      "s_cbranch_scc1 L_pace108\n"
      "s_cmpk_ge_i32 s31, 1721\n"
      "s_cselect_b64 vcc, -1, 0\n"
      "s_cbranch_vccnz L_go108\n"
      "s_waitcnt vmcnt(7)\n"
      "s_cmpk_ge_i32 s31, 1661\n"
      "s_cbranch_scc1 L_go108\n"
      "s_waitcnt vmcnt(6)\n"
      "L_go108:\n"
      "s_add_u32 m0, s92, 30464\n"
      "s_xor_b32 s85, s86, 99328\n"
      "buffer_load_dwordx4 v12, s[36:39], s85 offen nt lds\n"
      "s_waitcnt vmcnt(10)\n"
      "s_cbranch_vccnz L_go109\n"
      "s_waitcnt vmcnt(7)\n"
      "s_cmpk_ge_i32 s31, 1661\n"
      "s_cbranch_scc1 L_go109\n"
      "s_waitcnt vmcnt(6)\n"
      "L_go109:\n"
      "s_add_u32 m0, s92, 31552\n"
      "s_xor_b32 s85, s86, 103424\n"
      "buffer_load_dwordx4 v12, s[36:39], s85 offen nt lds\n"
      "s_waitcnt vmcnt(10)\n"
      "L_pace110:\n"
      "s_memrealtime s[30:31]\n"
      "s_waitcnt lgkmcnt(0)\n"
      "s_sub_u32 s31, s30, s4\n"
      "s_cmpk_lt_i32 s31, 1648\n"
      "s_cbranch_scc1 L_pace110\n"
      "s_cmpk_ge_i32 s31, 1748\n"
      "s_cselect_b64 vcc, -1, 0\n"
      "s_cbranch_vccnz L_go110\n"
      "s_waitcnt vmcnt(7)\n"
      "s_cmpk_ge_i32 s31, 1688\n"
      "s_cbranch_scc1 L_go110\n"
      "s_waitcnt vmcnt(6)\n"
      "L_go110:\n"
      "s_add_u32 m0, s92, 32640\n"
      "s_xor_b32 s85, s86, 107520\n"
      "buffer_load_dwordx4 v12, s[36:39], s85 offen nt lds\n"
      "s_waitcnt vmcnt(10)\n"
      "s_cbranch_vccnz L_go111\n"
      "s_waitcnt vmcnt(7)\n"
      "s_cmpk_ge_i32 s31, 1688\n"
      "s_cbranch_scc1 L_go111\n"
      "s_waitcnt vmcnt(6)\n"
      "L_go111:\n"
      "s_add_u32 m0, s92, 33728\n"
      "s_xor_b32 s85, s86, 111616\n"
      "buffer_load_dwordx4 v12, s[36:39], s85 offen nt lds\n"
      "s_waitcnt vmcnt(10)\n"
      "ds_read_b64_tr_b16 v[196:197], v3 offset:21760\n"
      "ds_read_b64_tr_b16 v[204:205], v4 offset:21760\n"
      "ds_read_b64_tr_b16 v[212:213], v3 offset:21824\n"
      "ds_read_b64_tr_b16 v[220:221], v4 offset:21824\n"
      "ds_read_b64_tr_b16 v[228:229], v3 offset:21888\n"
      "ds_read_b64_tr_b16 v[236:237], v4 offset:21888\n"
      "ds_read_b64_tr_b16 v[244:245], v3 offset:21952\n"
      "ds_read_b64_tr_b16 v[252:253], v4 offset:21952\n"
      "L_pace112:\n"
      "s_memrealtime s[30:31]\n"
      "s_waitcnt lgkmcnt(0)\n"
      "s_sub_u32 s31, s30, s4\n"
      "s_cmpk_lt_i32 s31, 1676\n"
      "s_cbranch_scc1 L_pace112\n"
      "s_cmpk_ge_i32 s31, 1776\n"
      "s_cselect_b64 vcc, -1, 0\n"
      "s_cbranch_vccnz L_go112\n"
      "s_waitcnt vmcnt(7)\n"
      "s_cmpk_ge_i32 s31, 1716\n"
      "s_cbranch_scc1 L_go112\n"
      "s_waitcnt vmcnt(6)\n"
      "L_go112:\n"
      "s_add_u32 m0, s92, 17408\n"
      "s_xor_b32 s85, s86, 132096\n"
      "buffer_load_dwordx4 v12, s[36:39], s85 offen nt lds\n"
      "s_waitcnt vmcnt(10)\n"
      "s_cbranch_vccnz L_go113\n"
      "s_waitcnt vmcnt(7)\n"
      "s_cmpk_ge_i32 s31, 1716\n"
      "s_cbranch_scc1 L_go113\n"
      "s_waitcnt vmcnt(4)\n"
      "L_go113:\n"
      "s_add_u32 m0, s92, 18496\n"
      "s_xor_b32 s85, s86, 136192\n"
      "buffer_load_dwordx4 v12, s[36:39], s85 offen nt lds\n"
      "s_waitcnt vmcnt(10)\n"
      "L_pace114:\n"
      "s_memrealtime s[30:31]\n"
      "s_waitcnt lgkmcnt(0)\n"
      "s_sub_u32 s31, s30, s4\n"
      "s_cmpk_lt_i32 s31, 1703\n"
      "s_cbranch_scc1 L_pace114\n"
      "s_cmpk_ge_i32 s31, 1803\n"
      "s_cselect_b64 vcc, -1, 0\n"
      "s_cbranch_vccnz L_go114\n"
      "s_waitcnt vmcnt(5)\n"
      "s_cmpk_ge_i32 s31, 1743\n"
      "s_cbranch_scc1 L_go114\n"
      "s_waitcnt vmcnt(4)\n"
      "L_go114:\n"
      "s_add_u32 m0, s92, 19584\n"
      "s_xor_b32 s85, s86, 140288\n"
      "buffer_load_dwordx4 v12, s[36:39], s85 offen nt lds\n"
      "s_waitcnt vmcnt(10)\n"
      "s_cbranch_vccnz L_go115\n"
      "s_waitcnt vmcnt(5)\n"
      "s_cmpk_ge_i32 s31, 1743\n"
      "s_cbranch_scc1 L_go115\n"
      "s_waitcnt vmcnt(4)\n"
      "L_go115:\n"
      "s_add_u32 m0, s92, 20672\n"
      "s_xor_b32 s85, s86, 144384\n"
      "buffer_load_dwordx4 v12, s[36:39], s85 offen nt lds\n"
      "s_waitcnt vmcnt(10)\n"
      "ds_read_b64_tr_b16 v[194:195], v3 offset:26112\n"
      "ds_read_b64_tr_b16 v[202:203], v4 offset:26112\n"
      "ds_read_b64_tr_b16 v[210:211], v3 offset:26176\n"
      "ds_read_b64_tr_b16 v[218:219], v4 offset:26176\n"
      "ds_read_b64_tr_b16 v[226:227], v3 offset:26240\n"
      "ds_read_b64_tr_b16 v[234:235], v4 offset:26240\n"
      "ds_read_b64_tr_b16 v[242:243], v3 offset:26304\n"
      "ds_read_b64_tr_b16 v[250:251], v4 offset:26304\n"
      "L_pace116:\n"
      "s_memrealtime s[30:31]\n"
      "s_waitcnt lgkmcnt(0)\n"
      "s_sub_u32 s31, s30, s4\n"
      "s_cmpk_lt_i32 s31, 1730\n"
      "s_cbranch_scc1 L_pace116\n"
      "s_cmpk_ge_i32 s31, 1830\n"
      "s_cselect_b64 vcc, -1, 0\n"
      "s_cbranch_vccnz L_go116\n"
      "s_waitcnt vmcnt(5)\n"
      "s_cmpk_ge_i32 s31, 1770\n"
      "s_cbranch_scc1 L_go116\n"
      "s_waitcnt vmcnt(4)\n"
      "L_go116:\n"
      "s_add_u32 m0, s92, 21760\n"
      "s_xor_b32 s85, s86, 197632\n"
      "buffer_load_dwordx4 v12, s[36:39], s85 offen nt lds\n"
      "s_waitcnt vmcnt(8)\n"
      "s_cbranch_vccnz L_go117\n"
      "s_waitcnt vmcnt(5)\n"
      "s_cmpk_ge_i32 s31, 1770\n"
      "s_cbranch_scc1 L_go117\n"
      "s_waitcnt vmcnt(4)\n"
      "L_go117:\n"
      "s_add_u32 m0, s92, 22848\n"
      "s_xor_b32 s85, s86, 201728\n"
      "buffer_load_dwordx4 v12, s[36:39], s85 offen nt lds\n"
      "s_waitcnt vmcnt(8)\n"
      "L_pace118:\n"
      "s_memrealtime s[30:31]\n"
      "s_waitcnt lgkmcnt(0)\n"
      "s_sub_u32 s31, s30, s4\n"
      "s_cmpk_lt_i32 s31, 1757\n"
      "s_cbranch_scc1 L_pace118\n"
      "s_cmpk_ge_i32 s31, 1857\n"
      "s_cselect_b64 vcc, -1, 0\n"
      "s_cbranch_vccnz L_go118\n"
      "s_waitcnt vmcnt(5)\n"
      "s_cmpk_ge_i32 s31, 1797\n"
      "s_cbranch_scc1 L_go118\n"
      "s_waitcnt vmcnt(4)\n"
      "L_go118:\n"
      "s_add_u32 m0, s92, 23936\n"
      "s_xor_b32 s85, s86, 205824\n"
      "buffer_load_dwordx4 v12, s[36:39], s85 offen nt lds\n"
      "s_waitcnt vmcnt(8)\n"
      "s_cbranch_vccnz L_go119\n"
      "s_waitcnt vmcnt(5)\n"
      "s_cmpk_ge_i32 s31, 1797\n"
      "s_cbranch_scc1 L_go119\n"
      "s_waitcnt vmcnt(4)\n"
      "L_go119:\n"
      "s_add_u32 m0, s92, 25024\n"
      "s_xor_b32 s85, s86, 209920\n"
      "buffer_load_dwordx4 v12, s[36:39], s85 offen nt lds\n"
      "s_waitcnt vmcnt(8)\n"
      "ds_read_b64_tr_b16 v[198:199], v3 offset:30464\n"
      "ds_read_b64_tr_b16 v[206:207], v4 offset:30464\n"
      "ds_read_b64_tr_b16 v[214:215], v3 offset:30528\n"
      "ds_read_b64_tr_b16 v[222:223], v4 offset:30528\n"
      "ds_read_b64_tr_b16 v[230:231], v3 offset:30592\n"
      "ds_read_b64_tr_b16 v[238:239], v4 offset:30592\n"
      "ds_read_b64_tr_b16 v[246:247], v3 offset:30656\n"
      "ds_read_b64_tr_b16 v[254:255], v4 offset:30656\n"
      "L_pace120:\n"
      "s_memrealtime s[30:31]\n"
      "s_waitcnt lgkmcnt(0)\n"
      "s_sub_u32 s31, s30, s4\n"
      "s_cmpk_lt_i32 s31, 1784\n"
      "s_cbranch_scc1 L_pace120\n"
      "s_cmpk_ge_i32 s31, 1884\n"
      "s_cselect_b64 vcc, -1, 0\n"
      "s_cbranch_vccnz L_go120\n"
      "s_waitcnt vmcnt(5)\n"
      "s_cmpk_ge_i32 s31, 1824\n"
      "s_cbranch_scc1 L_go120\n"
      "s_waitcnt vmcnt(4)\n"
      "L_go120:\n"
      "s_add_u32 m0, s92, 26112\n"
      "s_xor_b32 s85, s86, 164864\n"
      "buffer_load_dwordx4 v12, s[36:39], s85 offen nt lds\n"
      "s_waitcnt vmcnt(8)\n"
      "s_cbranch_vccnz L_go121\n"
      "s_waitcnt vmcnt(5)\n"
      "s_cmpk_ge_i32 s31, 1824\n"
      "s_cbranch_scc1 L_go121\n"
      "s_waitcnt vmcnt(4)\n"
      "L_go121:\n"
      "s_add_u32 m0, s92, 27200\n"
      "s_xor_b32 s85, s86, 168960\n"
      "buffer_load_dwordx4 v12, s[36:39], s85 offen nt lds\n"
      "s_waitcnt vmcnt(8)\n"
      "L_pace122:\n"
      "s_memrealtime s[30:31]\n"
      "s_waitcnt lgkmcnt(0)\n"
      "s_sub_u32 s31, s30, s4\n"
      "s_cmpk_lt_i32 s31, 1812\n"
      "s_cbranch_scc1 L_pace122\n"
      "s_cmpk_ge_i32 s31, 1912\n"
      "s_cselect_b64 vcc, -1, 0\n"
      "s_cbranch_vccnz L_go122\n"
      "s_waitcnt vmcnt(5)\n"
      "s_cmpk_ge_i32 s31, 1852\n"
      "s_cbranch_scc1 L_go122\n"
      "s_waitcnt vmcnt(4)\n"
      "L_go122:\n"
      "s_add_u32 m0, s92, 28288\n"
      "s_xor_b32 s85, s86, 173056\n"
      "buffer_load_dwordx4 v12, s[36:39], s85 offen nt lds\n"
      "s_waitcnt vmcnt(8)\n"
      "s_cbranch_vccnz L_go123\n"
      "s_waitcnt vmcnt(5)\n"
      "s_cmpk_ge_i32 s31, 1852\n"
      "s_cbranch_scc1 L_go123\n"
      "s_waitcnt vmcnt(4)\n"
      "L_go123:\n"
      "s_add_u32 m0, s92, 29376\n"
      "s_xor_b32 s85, s86, 177152\n"
      "buffer_load_dwordx4 v12, s[36:39], s85 offen nt lds\n"
      "s_waitcnt vmcnt(8)\n"
      "L_pace124:\n"
      "s_memrealtime s[30:31]\n"
      "s_waitcnt lgkmcnt(0)\n"
      "s_sub_u32 s31, s30, s4\n"
      "s_cmpk_lt_i32 s31, 1839\n"
      "s_cbranch_scc1 L_pace124\n"
      "s_cmpk_ge_i32 s31, 1939\n"
      "s_cselect_b64 vcc, -1, 0\n"
      "s_cbranch_vccnz L_go124\n"
      "s_waitcnt vmcnt(5)\n"
      "s_cmpk_ge_i32 s31, 1879\n"
      "s_cbranch_scc1 L_go124\n"
      "s_waitcnt vmcnt(4)\n"
      "L_go124:\n"
      "s_add_u32 m0, s92, 30464\n"
      "s_xor_b32 s85, s86, 230400\n"
      "buffer_load_dwordx4 v12, s[36:39], s85 offen nt lds\n"
      "s_waitcnt vmcnt(8)\n"
      "s_cbranch_vccnz L_go125\n"
      "s_waitcnt vmcnt(5)\n"
      "s_cmpk_ge_i32 s31, 1879\n"
      "s_cbranch_scc1 L_go125\n"
      "s_waitcnt vmcnt(4)\n"
      "L_go125:\n"
      "s_add_u32 m0, s92, 31552\n"
      "s_xor_b32 s85, s86, 234496\n"
      "buffer_load_dwordx4 v12, s[36:39], s85 offen nt lds\n"
      "L_pace126:\n"
      "s_memrealtime s[30:31]\n"
      "s_waitcnt lgkmcnt(0)\n"
      "s_sub_u32 s31, s30, s4\n"
      "s_cmpk_lt_i32 s31, 1866\n"
      "s_cbranch_scc1 L_pace126\n"
      "s_cmpk_ge_i32 s31, 1966\n"
      "s_cselect_b64 vcc, -1, 0\n"
      "s_add_u32 m0, s92, 32640\n"
      "s_xor_b32 s85, s86, 238592\n"
      "buffer_load_dwordx4 v12, s[36:39], s85 offen nt lds\n"
      "s_add_u32 m0, s92, 33728\n"
      "s_xor_b32 s85, s86, 242688\n"
      "buffer_load_dwordx4 v12, s[36:39], s85 offen nt lds\n"
      "s_barrier\n"
      "s_sleep 8\n"
      "s_lshl_b32 m0, s77, 12\n"
      "s_add_u32 m0, m0, 139264\n"
      "s_mov_b32 s85, 0\n"
      "buffer_load_dwordx4 v15, s[52:55], s85 offen   lds\n"
      "s_lshl_b32 m0, s77, 12\n"
      "s_add_u32 m0, m0, 140288\n"
      "s_mov_b32 s85, 8192\n"
      "buffer_load_dwordx4 v15, s[52:55], s85 offen   lds\n"
      "s_lshl_b32 m0, s77, 12\n"
      "s_add_u32 m0, m0, 141312\n"
      "s_mov_b32 s85, 16384\n"
      "buffer_load_dwordx4 v15, s[52:55], s85 offen   lds\n"
      "s_lshl_b32 m0, s77, 12\n"
      "s_add_u32 m0, m0, 142336\n"
      "s_mov_b32 s85, 24576\n"
      "buffer_load_dwordx4 v15, s[52:55], s85 offen   lds\n"
      "s_waitcnt lgkmcnt(0)\n"
      "v_smfmac_f32_16x16x64_bf16 v[48:51], v[40:43], v[192:199], v1\n"
      "v_smfmac_f32_16x16x64_bf16 v[52:55], v[40:43], v[200:207], v1\n"
      "v_smfmac_f32_16x16x64_bf16 v[56:59], v[40:43], v[208:215], v1\n"
      "v_smfmac_f32_16x16x64_bf16 v[60:63], v[40:43], v[216:223], v1\n"
      "v_smfmac_f32_16x16x64_bf16 v[64:67], v[40:43], v[224:231], v1\n"
      "v_smfmac_f32_16x16x64_bf16 v[68:71], v[40:43], v[232:239], v1\n"
      "v_smfmac_f32_16x16x64_bf16 v[72:75], v[40:43], v[240:247], v1\n"
      "v_smfmac_f32_16x16x64_bf16 v[76:79], v[40:43], v[248:255], v1\n"
      "v_mov_b64 v[40:41], -1\n"
      "v_mov_b64 v[42:43], -1\n"
      "v_add_f32_dpp v11, v11, v11 quad_perm:[2,3,0,1] row_mask:0xf bank_mask:0xf\n"
      "v_log_f32 v15, v11\n"
      "s_nop 0\n"
      "v_add_f32 v12, v8, v15\n"
      "v_min_u32 v12, -2, v12\n"
      "v_writelane_b32 v14, s68, 2\n"
      "s_waitcnt vmcnt(8)\n"
      "ds_read_b64_tr_b16 v[16:17], v3 offset:17408\n"
      "ds_read_b64_tr_b16 v[20:21], v3 offset:21760\n"
      "ds_read_b64_tr_b16 v[18:19], v3 offset:26112\n"
      "ds_read_b64_tr_b16 v[24:25], v4 offset:17408\n"
      "ds_read_b64_tr_b16 v[28:29], v4 offset:21760\n"
      "ds_read_b64_tr_b16 v[26:27], v4 offset:26112\n"
      "ds_read_b64_tr_b16 v[32:33], v3 offset:17472\n"
      "ds_read_b64_tr_b16 v[36:37], v3 offset:21824\n"
      "ds_read_b64_tr_b16 v[34:35], v3 offset:26176\n"
      "s_waitcnt vmcnt(6)\n"
      "L_spin1:\n"
      "v_mov_b32 v0, v0\n"
      "s_getreg_b32 s97, hwreg(HW_REG_IB_STS, 0, 4)\n"
      "s_cmp_gt_u32 s97, 4\n"
      "v_mov_b32 v0, v0\n"
      "s_cbranch_scc1 L_spin1\n"
      "s_waitcnt vmcnt(4)\n"
      "s_barrier\n"
      "s_memrealtime s[30:31]\n"
      "ds_read_b64_tr_b16 v[22:23], v3 offset:30464\n"
      "ds_read_b64_tr_b16 v[30:31], v4 offset:30464\n"
      "ds_read_b64_tr_b16 v[38:39], v3 offset:30528\n"
      "v_mov_b32 v0, v0\n"
      "s_nop 1\n"
      "v_mov_b32 v0, v0\n"
      "s_nop 1\n"
      "v_mov_b32 v0, v0\n"
      "s_waitcnt lgkmcnt(0)\n"
      "s_add_u32 s6, s30, 30\n"
      "s_min_u32 s6, s6, -2\n"
      "v_writelane_b32 v12, s6, 1\n"
      "v_writelane_b32 v12, s6, 2\n"
      "buffer_store_dword v12, v14, s[40:43], 0 offen sc1\n"
      "s_branch L_pv3\n"
      "L_lead:\n"
      "v_min_u32 v56, v192, v196\n"
      "v_min3_u32 v56, v56, v200, v204\n"
      "v_cmp_eq_u32 vcc, 0, v56\n"
      "s_cbranch_vccz L_lead_grant\n"
      "s_mov_b32 s93, 1720320\n"
      "buffer_load_dword v192, v58, s[40:43], s93 offen offset:0 sc1\n"
      "s_mov_b32 s93, 1722368\n"
      "buffer_load_dword v196, v58, s[40:43], s93 offen offset:0 sc1\n"
      "s_mov_b32 s93, 1724416\n"
      "buffer_load_dword v200, v58, s[40:43], s93 offen offset:0 sc1\n"
      "s_mov_b32 s93, 1726464\n"
      "buffer_load_dword v204, v58, s[40:43], s93 offen offset:0 sc1\n"
      "s_setpc_b64 s[66:67]\n"
      "L_lead_grant:\n"
      "s_lshl_b32 s94, s70, 12\n"
      "v_subrev_u32 v193, s94, v192\n"
      "v_ashrrev_i32 v193, 12, v193\n"
      "v_subrev_u32 v197, s94, v196\n"
      "v_ashrrev_i32 v197, 12, v197\n"
      "v_subrev_u32 v201, s94, v200\n"
      "v_ashrrev_i32 v201, 12, v201\n"
      "v_subrev_u32 v205, s94, v204\n"
      "v_ashrrev_i32 v205, 12, v205\n"
      "v_add_u32 v198, v193, v197\n"
      "v_add3_u32 v198, v198, v201, v205\n"
      "s_nop 1\n"
      "v_add_u32_dpp v198, v198, v198 quad_perm:[1,0,3,2] row_mask:0xf bank_mask:0xf\n"
      "s_nop 1\n"
      "v_add_u32_dpp v198, v198, v198 quad_perm:[2,3,0,1] row_mask:0xf bank_mask:0xf\n"
      "s_nop 1\n"
      "v_add_u32_dpp v198, v198, v198 row_half_mirror row_mask:0xf bank_mask:0xf\n"
      "s_nop 1\n"
      "v_add_u32_dpp v198, v198, v198 row_mirror row_mask:0xf bank_mask:0xf\n"
      "s_nop 1\n"
      "v_add_u32_dpp v198, v198, v198 row_bcast:15 row_mask:0xa bank_mask:0xf\n"
      "s_nop 1\n"
      "v_add_u32_dpp v198, v198, v198 row_bcast:31 row_mask:0xc bank_mask:0xf\n"
      "s_nop 0\n"
      "v_readlane_b32 s95, v198, 63\n"
      "v_add_u32 v192, v192, v196\n"
      "v_add3_u32 v192, v192, v200, v204\n"
      "v_and_b32 v192, 15, v192\n"
      "s_nop 1\n"
      "v_add_u32_dpp v192, v192, v192 row_ror:4 row_mask:0xf bank_mask:0xf\n"
      "s_nop 1\n"
      "v_add_u32_dpp v192, v192, v192 row_ror:8 row_mask:0xf bank_mask:0xf\n"
      "v_mov_b32 v193, v192\n"
      "s_nop 1\n"
      "v_permlane16_swap_b32 v192, v193\n"
      "v_add_u32 v192, v192, v193\n"
      "v_mov_b32 v193, v192\n"
      "s_nop 1\n"
      "v_permlane32_swap_b32 v192, v193\n"
      "v_add_u32 v192, v192, v193\n"
      "v_cmp_lt_u32 vcc, 96, v192\n"
      "s_nop 1\n"
      "s_and_b32 s93, vcc_lo, 15\n"
      "s_or_b32 s93, s93, 0x100\n"
      "s_ashr_i32 s95, s95, 8\n"
      "s_add_u32 s94, s70, s95\n"
      "s_lshl_b32 s94, s94, 9\n"
      "s_or_b32 s93, s93, s94\n"
      "v_mov_b32 v208, s93\n"
      "s_mov_b32 s6, 0\n"
      "s_mov_b32 s93, 1720320\n"
      "buffer_store_dword v208, v58, s[40:43], s93 offen offset:4 sc1\n"
      "s_mov_b32 s93, 1722368\n"
      "buffer_store_dword v208, v58, s[40:43], s93 offen offset:4 sc1\n"
      "s_mov_b32 s93, 1724416\n"
      "buffer_store_dword v208, v58, s[40:43], s93 offen offset:4 sc1\n"
      "s_mov_b32 s93, 1726464\n"
      "buffer_store_dword v208, v58, s[40:43], s93 offen offset:4 sc1\n"
      "s_setpc_b64 s[66:67]\n"
      "L_pend_d:\n"
      ".p2align 12\n"
      "L_stub_d:\n"
      "s_setpc_b64 s[66:67]\n"
      "L_over_d:\n"
      "L_delta:\n"
      "v_mov_b32 v12, 163392\n"
      "ds_read_b32 v12, v12\n"
      "v_lshlrev_b32 v10, 4, v0\n"
      "v_lshrrev_b32 v11, 2, v0\n"
      "v_lshlrev_b32 v11, 7, v11\n"
      "v_and_b32 v15, 1, v0\n"
      "v_lshl_add_u32 v11, v15, 2, v11\n"
      "s_mov_b32 s87, 165\n"
      "s_sub_u32 s31, s7, s70\n"
      "s_add_u32 s87, s87, 22\n"
      "s_waitcnt lgkmcnt(0)\n"
      "v_readfirstlane_b32 s30, v12\n"
      "s_cmp_ge_i32 s30, s31\n"
      "s_cselect_b32 s30, s30, s31\n"
      "s_cselect_b32 s92, 0, 22\n"
      "s_add_u32 s7, s30, s70\n"
      "s_add_u32 s87, s87, s7\n"
      "s_sub_u32 s87, s87, s92\n"
      "L_de_clk:\n"
      "s_memrealtime s[30:31]\n"
      "s_waitcnt lgkmcnt(0)\n"
      "s_sub_u32 s30, s30, s87\n"
      "L_de_clk_in:\n"
      "s_cmp_lt_i32 s30, -4\n"
      "s_cbranch_scc1 L_de_clk\n"
      "s_cmp_ge_i32 s30, -1\n"
      "s_cbranch_scc1 L_de_clk_go\n"
      "s_nop 3\n"
      "s_cmp_ge_i32 s30, -2\n"
      "s_cbranch_scc1 L_de_clk_go\n"
      "s_nop 3\n"
      "s_cmp_ge_i32 s30, -3\n"
      "s_cbranch_scc1 L_de_clk_go\n"
      "s_nop 7\n"
      "L_de_clk_go:\n"
      "buffer_load_dword v13, v11, s[40:43], s73 offen sc1\n"
      "buffer_load_dwordx4 v[32:35], v10, s[40:43], s72 offen sc1\n"
      "v_and_b32 v88, 15, v0\n"
      "v_lshlrev_b32 v88, 4, v88\n"
      "v_bfe_u32 v15, v0, 4, 1\n"
      "v_lshl_add_u32 v88, v15, 3, v88\n"
      "v_lshlrev_b32 v89, 4, v0\n"
      "v_and_b32 v90, 0x30, v89\n"
      "v_and_b32 v15, 7, v0\n"
      "v_lshlrev_b32 v15, 4, v15\n"
      "v_add_u32 v15, 162752, v15\n"
      "ds_read_b128 v[36:39], v15\n"
      "v_and_b32 v14, 3, v0\n"
      "v_lshlrev_b32 v14, 5, v14\n"
      "v_and_b32 v15, 16, v0\n"
      "v_add_u32 v14, v15, v14\n"
      "v_lshrrev_b32 v15, 5, v0\n"
      "v_lshl_add_u32 v14, v15, 2, v14\n"
      "v_bfe_u32 v15, v0, 2, 1\n"
      "v_lshl_add_u32 v14, v15, 1, v14\n"
      "s_lshr_b32 s95, s77, 1\n"
      "s_lshl_b32 s95, s95, 7\n"
      "s_and_b32 s97, s77, 1\n"
      "s_lshl_b32 s97, s97, 3\n"
      "s_add_u32 s95, s95, s97\n"
      "s_add_u32 s95, s95, 160960\n"
      "v_add_u32 v14, s95, v14\n"
      "ds_read_b128 v[16:19], v2 offset:0\n"
      "ds_read_b128 v[20:23], v2 offset:64\n"
      "v_mov_b64 v[48:49], 0\n"
      "ds_read_b128 v[24:27], v2 offset:4352\n"
      "ds_read_b128 v[28:31], v2 offset:4416\n"
      "v_mov_b64 v[52:53], 0\n"
      "ds_read_b128 v[64:67], v2 offset:8704\n"
      "ds_read_b128 v[68:71], v2 offset:8768\n"
      "v_mov_b64 v[56:57], 0\n"
      "ds_read_b128 v[72:75], v2 offset:13056\n"
      "ds_read_b128 v[76:79], v2 offset:13120\n"
      "v_mov_b64 v[60:61], 0\n"
      "v_bfe_u32 v7, v0, 2, 2\n"
      "v_mul_u32_u24 v7, 320, v7\n"
      "v_sub_u32 v7, 160960, v7\n"
      "v_lshrrev_b32 v15, 4, v0\n"
      "v_lshl_add_u32 v7, v15, 5, v7\n"
      "v_and_b32 v15, 1, v0\n"
      "v_lshl_add_u32 v7, v15, 4, v7\n"
      "s_lshl_b32 s95, s77, 9\n"
      "v_and_b32 v9, 0x39, v0\n"
      "v_lshlrev_b32 v9, 3, v9\n"
      "v_and_b32 v15, 2, v0\n"
      "v_lshl_add_u32 v9, v15, 4, v9\n"
      "v_and_b32 v15, 4, v0\n"
      "v_lshl_add_u32 v9, v15, 2, v9\n"
      "s_add_u32 s95, s95, 155648\n"
      "v_add_u32 v9, s95, v9\n"
      "v_bfe_u32 v92, v0, 3, 2\n"
      "s_lshl_b32 s95, s77, 1\n"
      "v_xor_b32 v92, s95, v92\n"
      "v_and_b32 v92, 3, v92\n"
      "v_lshlrev_b32 v92, 5, v92\n"
      "v_and_b32 v93, 0x30, v0\n"
      "v_lshl_add_u32 v92, v93, 8, v92\n"
      "v_and_b32 v93, 12, v0\n"
      "v_lshl_add_u32 v92, v93, 5, v92\n"
      "v_and_b32 v93, 3, v0\n"
      "v_lshl_add_u32 v92, v93, 3, v92\n"
      "v_add_u32 v92, 139264, v92\n"
      "v_xor_b32 v93, 32, v92\n"
      "v_lshl_or_b32 v50, v0, 25, s68\n"
      "s_and_b32 s95, s68, 0x80000000\n"
      "s_lshl_b32 s97, s76, 5\n"
      "s_or_b32 s95, s95, s97\n"
      "s_add_u32 s95, s95, 196608\n"
      "v_lshrrev_b32 v15, 1, v0\n"
      "v_lshl_or_b32 v91, v15, 25, s95\n"
      "v_and_b32 v15, 1, v0\n"
      "v_lshl_add_u32 v91, v15, 13, v91\n"
      "s_waitcnt lgkmcnt(0)\n"
      "L_spin2:\n"
      "v_mov_b32 v0, v0\n"
      "s_getreg_b32 s97, hwreg(HW_REG_IB_STS, 0, 4)\n"
      "s_cmp_gt_u32 s97, 1\n"
      "v_mov_b32 v0, v0\n"
      "s_cbranch_scc1 L_spin2\n"
      "s_waitcnt vmcnt(1)\n"
      "L_dg_chk:\n"
      "v_add_f32_dpp v12, v13, v13 row_ror:4 row_mask:0xf bank_mask:0xf\n"
      "s_waitcnt vmcnt(0)\n"
      "v_mfma_f32_16x16x32_bf16 v[40:43], v[32:35], v[36:39], 0\n"
      "v_add_f32_dpp v12, v12, v12 row_ror:8 row_mask:0xf bank_mask:0xf\n"
      "v_mov_b32 v8, v12\n"
      "v_max3_u32 v15, v13, v32, v33\n"
      "v_max3_u32 v15, v15, v34, v35\n"
      "v_permlane16_swap_b32 v12, v8\n"
      "v_add_f32 v12, v12, v8\n"
      "v_mov_b32 v8, v12\n"
      "v_cmp_eq_u32 vcc, -1, v15\n"
      "v_permlane32_swap_b32 v40, v42\n"
      "v_permlane32_swap_b32 v41, v43\n"
      "v_add_f32 v40, v40, v42\n"
      "v_add_f32 v41, v41, v43\n"
      "v_permlane32_swap_b32 v12, v8\n"
      "v_add_f32 v12, v12, v8\n"
      "v_permlane16_swap_b32 v40, v41\n"
      "v_add_f32 v40, v40, v41\n"
      "v_mul_f32_dpp v8, v12, v6 quad_perm:[0,0,0,0] row_mask:0xf bank_mask:0xf\n"
      "v_add_f32 v40, v40, v8\n"
      "v_cvt_pk_bf16_f32 v40, v40, v40\n"
      "s_cbranch_vccnz L_dg_retry\n"
      "L_dg_ok:\n"
      "ds_write_b16 v14, v40\n"
      "s_memrealtime s[30:31]\n"
      "v_mov_b32 v13, 0x3b000000\n"
      "v_mov_b32 v15, 0x3727c5ac\n"
      "s_nop 0\n"
      "v_mul_f32_dpp v14, v12, v13 quad_perm:[0,0,0,0] row_mask:0xf bank_mask:0xf\n"
      "v_fma_f32 v14, -v14, v14, v15\n"
      "s_nop 1\n"
      "v_fmac_f32_dpp v14, v12, v13 quad_perm:[1,1,1,1] row_mask:0xf bank_mask:0xf\n"
      "v_max_f32 v14, v14, v15\n"
      "v_rsq_f32 v14, v14\n"
      "s_nop 0\n"
      "v_mul_f32 v8, 0x3e0293ee, v14\n"
      "s_waitcnt lgkmcnt(0)\n"
      "v_mov_b32 v0, v0\n"
      "s_barrier\n"
      "L_att:\n"
      "ds_read_b128 v[32:35], v7 offset:960\n"
      "v_mov_b32 v0, v0\n"
      "ds_read_b128 v[36:39], v7 offset:0\n"
      "ds_read_b128 v[40:43], v7 offset:128\n"
      "ds_read_b128 v[44:47], v7 offset:320\n"
      "s_add_u32 s6, s30, 100\n"
      "s_min_u32 s6, s6, -2\n"
      "v_mov_b32 v13, s6\n"
      "buffer_store_dword v13, v50, s[40:43], 0 offen sc1\n"
      "v_mov_b32 v0, v0\n"
      "s_waitcnt lgkmcnt(3)\n"
      "v_smfmac_f32_16x16x64_bf16 v[48:51], v[32:35], v[16:23], v1\n"
      "ds_read_b128 v[16:19], v2 offset:128\n"
      "v_smfmac_f32_16x16x64_bf16 v[52:55], v[32:35], v[24:31], v1\n"
      "ds_read_b128 v[20:23], v2 offset:192\n"
      "v_smfmac_f32_16x16x64_bf16 v[56:59], v[32:35], v[64:71], v1\n"
      "ds_read_b128 v[24:27], v2 offset:4480\n"
      "v_smfmac_f32_16x16x64_bf16 v[60:63], v[32:35], v[72:79], v1\n"
      "s_waitcnt lgkmcnt(3)\n"
      "v_smfmac_f32_16x16x64_bf16 v[48:51], v[36:39], a[0:7], v1\n"
      "ds_read_b128 v[32:35], v7 offset:1088\n"
      "v_smfmac_f32_16x16x64_bf16 v[52:55], v[36:39], a[16:23], v1\n"
      "ds_read_b128 v[28:31], v2 offset:4544\n"
      "v_smfmac_f32_16x16x64_bf16 v[56:59], v[36:39], a[32:39], v1\n"
      "ds_read_b128 v[64:67], v2 offset:8832\n"
      "v_smfmac_f32_16x16x64_bf16 v[60:63], v[36:39], a[48:55], v1\n"
      "ds_read_b128 v[68:71], v2 offset:8896\n"
      "v_smfmac_f32_16x16x64_bf16 v[48:51], v[40:43], a[8:15], v1\n"
      "ds_read_b128 v[72:75], v2 offset:13184\n"
      "v_smfmac_f32_16x16x64_bf16 v[52:55], v[40:43], a[24:31], v1\n"
      "ds_read_b128 v[76:79], v2 offset:13248\n"
      "v_smfmac_f32_16x16x64_bf16 v[56:59], v[40:43], a[40:47], v1\n"
      "ds_read_b128 v[36:39], v7 offset:448\n"
      "v_smfmac_f32_16x16x64_bf16 v[60:63], v[40:43], a[56:63], v1\n"
      "v_and_b32 v12, 0x30, v0\n"
      "v_smfmac_f32_16x16x64_bf16 v[48:51], v[44:47], a[64:71], v1\n"
      "ds_read_b128 v[40:43], v7 offset:640\n"
      "v_smfmac_f32_16x16x64_bf16 v[52:55], v[44:47], a[80:87], v1\n"
      "v_add_u32 v12, v12, v0\n"
      "v_smfmac_f32_16x16x64_bf16 v[56:59], v[44:47], a[96:103], v1\n"
      "s_mul_i32 s95, s77, 576\n"
      "v_smfmac_f32_16x16x64_bf16 v[60:63], v[44:47], a[112:119], v1\n"
      "s_waitcnt lgkmcnt(1)\n"
      "v_smfmac_f32_16x16x64_bf16 v[48:51], v[32:35], v[16:23], v1\n"
      "ds_read_b128 v[44:47], v7 offset:768\n"
      "v_smfmac_f32_16x16x64_bf16 v[52:55], v[32:35], v[24:31], v1\n"
      "v_and_b32 v7, 3, v0\n"
      "v_smfmac_f32_16x16x64_bf16 v[56:59], v[32:35], v[64:71], v1\n"
      "v_lshlrev_b32 v7, 9, v7\n"
      "v_smfmac_f32_16x16x64_bf16 v[60:63], v[32:35], v[72:79], v1\n"
      "v_add_u32 v7, 156152, v7\n"
      "v_smfmac_f32_16x16x64_bf16 v[48:51], v[36:39], a[72:79], v1\n"
      "s_add_u32 s95, s95, 157696\n"
      "v_smfmac_f32_16x16x64_bf16 v[52:55], v[36:39], a[88:95], v1\n"
      "v_lshl_add_u32 v13, v12, 2, s95\n"
      "v_smfmac_f32_16x16x64_bf16 v[56:59], v[36:39], a[104:111], v1\n"
      "ds_read_b64_tr_b16 v[16:17], v3 offset:17408\n"
      "v_smfmac_f32_16x16x64_bf16 v[60:63], v[36:39], a[120:127], v1\n"
      "s_waitcnt lgkmcnt(1)\n"
      "v_smfmac_f32_16x16x64_bf16 v[48:51], v[40:43], a[128:135], v1\n"
      "ds_read_b64_tr_b16 v[20:21], v3 offset:21760\n"
      "v_smfmac_f32_16x16x64_bf16 v[52:55], v[40:43], a[144:151], v1\n"
      "ds_read_b64_tr_b16 v[18:19], v3 offset:26112\n"
      "v_smfmac_f32_16x16x64_bf16 v[56:59], v[40:43], a[160:167], v1\n"
      "ds_read_b64_tr_b16 v[22:23], v3 offset:30464\n"
      "v_smfmac_f32_16x16x64_bf16 v[60:63], v[40:43], a[176:183], v1\n"
      "ds_read_b64_tr_b16 v[24:25], v4 offset:17408\n"
      "v_smfmac_f32_16x16x64_bf16 v[48:51], v[44:47], a[136:143], v1\n"
      "ds_read_b64_tr_b16 v[28:29], v4 offset:21760\n"
      "v_smfmac_f32_16x16x64_bf16 v[52:55], v[44:47], a[152:159], v1\n"
      "ds_read_b64_tr_b16 v[26:27], v4 offset:26112\n"
      "v_smfmac_f32_16x16x64_bf16 v[56:59], v[44:47], a[168:175], v1\n"
      "ds_read_b64_tr_b16 v[30:31], v4 offset:30464\n"
      "v_smfmac_f32_16x16x64_bf16 v[60:63], v[44:47], a[184:191], v1\n"
      "s_cmp_eq_u32 s80, 0\n"
      "s_cselect_b64 s[98:99], 0, -1\n"
      "v_add_f32 v50, v48, v49\n"
      "v_mov_b64 v[48:49], 0\n"
      "v_add_f32 v54, v52, v53\n"
      "v_mov_b64 v[52:53], 0\n"
      "v_add_f32 v58, v56, v57\n"
      "v_max3_f32 v12, v50, v54, v58\n"
      "v_add_f32 v62, v60, v61\n"
      "v_max_f32 v12, v12, v62\n"
      "v_mov_b64 v[56:57], 0\n"
      "v_mov_b64 v[60:61], 0\n"
      "v_max_f32_dpp v12, v12, v12 quad_perm:[1,0,3,2] row_mask:0xf bank_mask:0xf\n"
      "v_mul_f32 v50, v50, v8\n"
      "v_mul_f32 v54, v54, v8\n"
      "v_max_f32_dpp v12, v12, v12 quad_perm:[2,3,0,1] row_mask:0xf bank_mask:0xf\n"
      "v_mul_f32 v58, v58, v8\n"
      "v_mul_f32 v62, v62, v8\n"
      "v_max_f32_dpp v12, v12, v12 row_half_mirror row_mask:0xf bank_mask:0xf\n"
      "v_mov_b64 v[64:65], 0\n"
      "v_mov_b64 v[68:69], 0\n"
      "v_max_f32_dpp v12, v12, v12 row_mirror row_mask:0xf bank_mask:0xf\n"
      "v_mov_b64 v[72:73], 0\n"
      "v_mov_b64 v[76:77], 0\n"
      "v_max_f32_dpp v12, v12, v12 row_bcast:15 row_mask:0xa bank_mask:0xf\n"
      "s_nop 1\n"
      "v_max_f32_dpp v12, v12, v12 row_bcast:31 row_mask:0xc bank_mask:0xf\n"
      "v_mul_f32 v10, v12, v8\n"
      "s_nop 0\n"
      "v_readlane_b32 s87, v10, 63\n"
      "s_nop 1\n"
      "v_subrev_f32 v50, s87, v50\n"
      "v_subrev_f32 v54, s87, v54\n"
      "v_subrev_f32 v58, s87, v58\n"
      "v_subrev_f32 v62, s87, v62\n"
      "v_exp_f32 v50, v50\n"
      "v_exp_f32 v54, v54\n"
      "v_exp_f32 v58, v58\n"
      "v_exp_f32 v62, v62\n"
      "v_cvt_pk_bf16_f32 v14, v50, v54\n"
      "v_cvt_pk_bf16_f32 v15, v58, v62\n"
      "ds_write_b64 v9, v[14:15]\n"
      "ds_read_b128 v[32:35], v5 offset:0\n"
      "v_add_f32 v11, v50, v54\n"
      "v_add_f32 v12, v58, v62\n"
      "v_add_f32 v11, v11, v12\n"
      "ds_read_b128 v[36:39], v5 offset:128\n"
      "ds_read_b128 v[40:43], v5 offset:256\n"
      "v_add_f32_dpp v11, v11, v11 quad_perm:[1,0,3,2] row_mask:0xf bank_mask:0xf\n"
      "ds_read_b128 v[44:47], v5 offset:384\n"
      "s_nop 0\n"
      "v_add_f32_dpp v11, v11, v11 quad_perm:[2,3,0,1] row_mask:0xf bank_mask:0xf\n"
      "s_nop 1\n"
      "v_add_f32_dpp v11, v11, v11 row_half_mirror row_mask:0xf bank_mask:0xf\n"
      "s_nop 1\n"
      "v_add_f32_dpp v11, v11, v11 row_mirror row_mask:0xf bank_mask:0xf\n"
      "s_nop 1\n"
      "v_add_f32_dpp v11, v11, v11 row_bcast:15 row_mask:0xa bank_mask:0xf\n"
      "s_waitcnt lgkmcnt(3)\n"
      "v_smfmac_f32_16x16x64_bf16 v[48:51], v[32:35], a[192:199], v1\n"
      "v_add_f32_dpp v11, v11, v11 row_bcast:31 row_mask:0xc bank_mask:0xf\n"
      "v_smfmac_f32_16x16x64_bf16 v[52:55], v[32:35], a[200:207], v1\n"
      "ds_write_b64 v9, v[10:11]\n"
      "v_smfmac_f32_16x16x64_bf16 v[56:59], v[32:35], a[208:215], v1\n"
      "v_mov_b32 v9, v13\n"
      "v_smfmac_f32_16x16x64_bf16 v[60:63], v[32:35], a[216:223], v1\n"
      "v_mov_b32 v14, 0x80000000\n"
      "v_mov_b32 v12, -2\n"
      "v_smfmac_f32_16x16x64_bf16 v[64:67], v[32:35], a[224:231], v1\n"
      "s_add_u32 s95, s33, 4\n"
      "v_cndmask_b32 v15, v14, v91, s[98:99]\n"
      "v_smfmac_f32_16x16x64_bf16 v[68:71], v[32:35], a[232:239], v1\n"
      "s_waitcnt lgkmcnt(0)\n"
      "s_barrier\n"
      "ds_read_b64 v[10:11], v7\n"
      "v_smfmac_f32_16x16x64_bf16 v[72:75], v[32:35], a[240:247], v1\n"
      "s_memrealtime s[30:31]\n"
      "v_writelane_b32 v14, s33, 0\n"
      "v_smfmac_f32_16x16x64_bf16 v[76:79], v[32:35], a[248:255], v1\n"
      "v_writelane_b32 v14, s95, 1\n"
      "v_smfmac_f32_16x16x64_bf16 v[48:51], v[36:39], v[128:135], v1\n"
      "s_waitcnt lgkmcnt(0)\n"
      "v_max_f32_dpp v8, v10, v10 quad_perm:[1,0,3,2] row_mask:0xf bank_mask:0xf\n"
      "v_smfmac_f32_16x16x64_bf16 v[52:55], v[36:39], v[136:143], v1\n"
      "v_min_u32 v13, s30, v12\n"
      "buffer_store_dword v13, v15, s[40:43], 0 offen sc1\n"
      "v_smfmac_f32_16x16x64_bf16 v[56:59], v[36:39], v[144:151], v1\n"
      "v_max_f32_dpp v8, v8, v8 quad_perm:[2,3,0,1] row_mask:0xf bank_mask:0xf\n"
      "v_sub_f32 v10, v10, v8\n"
      "v_smfmac_f32_16x16x64_bf16 v[60:63], v[36:39], v[152:159], v1\n"
      "v_exp_f32 v10, v10\n"
      "v_smfmac_f32_16x16x64_bf16 v[64:67], v[36:39], v[160:167], v1\n"
      "v_mul_f32 v11, v10, v11\n"
      "v_smfmac_f32_16x16x64_bf16 v[68:71], v[36:39], v[168:175], v1\n"
      "s_cmp_eq_u32 s80, 0\n"
      "v_smfmac_f32_16x16x64_bf16 v[72:75], v[36:39], v[176:183], v1\n"
      "v_add_f32_dpp v11, v11, v11 quad_perm:[1,0,3,2] row_mask:0xf bank_mask:0xf\n"
      "v_smfmac_f32_16x16x64_bf16 v[76:79], v[36:39], v[184:191], v1\n"
      "s_cbranch_scc1 L_hook\n"
      "v_add_f32_dpp v11, v11, v11 quad_perm:[2,3,0,1] row_mask:0xf bank_mask:0xf\n"
      "v_smfmac_f32_16x16x64_bf16 v[48:51], v[40:43], v[192:199], v1\n"
      "v_log_f32 v15, v11\n"
      "v_smfmac_f32_16x16x64_bf16 v[52:55], v[40:43], v[200:207], v1\n"
      "v_add_f32 v12, v8, v15\n"
      "v_min_u32 v12, -2, v12\n"
      "v_smfmac_f32_16x16x64_bf16 v[56:59], v[40:43], v[208:215], v1\n"
      "buffer_store_dword v12, v14, s[40:43], 0 offen sc1\n"
      "v_smfmac_f32_16x16x64_bf16 v[60:63], v[40:43], v[216:223], v1\n"
      "ds_read_b64_tr_b16 v[32:33], v3 offset:17472\n"
      "buffer_load_dwordx2 v[80:81], v88, s[40:43], s48 offen sc1\n"
      "v_smfmac_f32_16x16x64_bf16 v[64:67], v[40:43], v[224:231], v1\n"
      "ds_read_b64_tr_b16 v[36:37], v3 offset:21824\n"
      "v_smfmac_f32_16x16x64_bf16 v[68:71], v[40:43], v[232:239], v1\n"
      "ds_read_b64_tr_b16 v[34:35], v3 offset:26176\n"
      "v_smfmac_f32_16x16x64_bf16 v[72:75], v[40:43], v[240:247], v1\n"
      "ds_read_b64_tr_b16 v[38:39], v3 offset:30528\n"
      "v_smfmac_f32_16x16x64_bf16 v[76:79], v[40:43], v[248:255], v1\n"
      "buffer_load_dword v82, v90, s[40:43], s86 offen sc1\n"
      "L_pv3:\n"
      "v_smfmac_f32_16x16x64_bf16 v[48:51], v[44:47], v[16:23], v1\n"
      "ds_read_b64_tr_b16 v[16:17], v4 offset:17472\n"
      "ds_read_b64_tr_b16 v[20:21], v4 offset:21824\n"
      "ds_read_b64_tr_b16 v[18:19], v4 offset:26176\n"
      "ds_read_b64_tr_b16 v[22:23], v4 offset:30528\n"
      "v_smfmac_f32_16x16x64_bf16 v[52:55], v[44:47], v[24:31], v1\n"
      "ds_read_b64_tr_b16 v[24:25], v3 offset:17536\n"
      "ds_read_b64_tr_b16 v[28:29], v3 offset:21888\n"
      "ds_read_b64_tr_b16 v[26:27], v3 offset:26240\n"
      "ds_read_b64_tr_b16 v[30:31], v3 offset:30592\n"
      "s_waitcnt lgkmcnt(8)\n"
      "v_smfmac_f32_16x16x64_bf16 v[56:59], v[44:47], v[32:39], v1\n"
      "ds_read_b64_tr_b16 v[32:33], v4 offset:17536\n"
      "ds_read_b64_tr_b16 v[36:37], v4 offset:21888\n"
      "ds_read_b64_tr_b16 v[34:35], v4 offset:26240\n"
      "ds_read_b64_tr_b16 v[38:39], v4 offset:30592\n"
      "s_waitcnt lgkmcnt(8)\n"
      "v_smfmac_f32_16x16x64_bf16 v[60:63], v[44:47], v[16:23], v1\n"
      "ds_read_b64_tr_b16 v[16:17], v3 offset:17600\n"
      "ds_read_b64_tr_b16 v[20:21], v3 offset:21952\n"
      "ds_read_b64_tr_b16 v[18:19], v3 offset:26304\n"
      "ds_read_b64_tr_b16 v[22:23], v3 offset:30656\n"
      "s_waitcnt lgkmcnt(8)\n"
      "v_smfmac_f32_16x16x64_bf16 v[64:67], v[44:47], v[24:31], v1\n"
      "ds_read_b64_tr_b16 v[24:25], v4 offset:17600\n"
      "ds_read_b64_tr_b16 v[28:29], v4 offset:21952\n"
      "ds_read_b64_tr_b16 v[26:27], v4 offset:26304\n"
      "ds_read_b64_tr_b16 v[30:31], v4 offset:30656\n"
      "s_waitcnt lgkmcnt(8)\n"
      "v_smfmac_f32_16x16x64_bf16 v[68:71], v[44:47], v[32:39], v1\n"
      "v_add_f32_dpp v14, v48, v49 quad_perm:[0,1,2,3] row_mask:0x1 bank_mask:0xf\n"
      "s_waitcnt lgkmcnt(4)\n"
      "v_smfmac_f32_16x16x64_bf16 v[72:75], v[44:47], v[16:23], v1\n"
      "v_add_f32_dpp v15, v52, v53 quad_perm:[0,1,2,3] row_mask:0x1 bank_mask:0xf\n"
      "s_waitcnt lgkmcnt(0)\n"
      "v_smfmac_f32_16x16x64_bf16 v[76:79], v[44:47], v[24:31], v1\n"
      "v_add_f32_dpp v14, v56, v57 quad_perm:[0,1,2,3] row_mask:0x2 bank_mask:0xf\n"
      "v_add_f32_dpp v15, v60, v61 quad_perm:[0,1,2,3] row_mask:0x2 bank_mask:0xf\n"
      "v_lshrrev_b32 v8, 4, v0\n"
      "v_add_f32_dpp v14, v64, v65 quad_perm:[0,1,2,3] row_mask:0x4 bank_mask:0xf\n"
      "v_add_f32_dpp v15, v68, v69 quad_perm:[0,1,2,3] row_mask:0x4 bank_mask:0xf\n"
      "v_xor_b32 v7, v8, v0\n"
      "v_add_f32_dpp v14, v72, v73 quad_perm:[0,1,2,3] row_mask:0x8 bank_mask:0xf\n"
      "v_and_b32 v7, 3, v7\n"
      "v_add_f32_dpp v15, v76, v77 quad_perm:[0,1,2,3] row_mask:0x8 bank_mask:0xf\n"
      "ds_write2_b32 v9, v14, v15 offset1:16\n"
      "s_lshl_b32 s95, s80, 31\n"
      "v_lshl_or_b32 v28, v7, 7, s95\n"
      "v_and_or_b32 v28, v0, 4, v28\n"
      "v_and_b32 v7, 8, v0\n"
      "v_lshl_or_b32 v28, v7, 24, v28\n"
      "buffer_load_dword v30, v28, s[40:43], s35 offen sc1\n"
      "v_and_b32 v13, 3, v0\n"
      "v_mul_u32_u24 v12, 576, v13\n"
      "v_and_b32 v7, 0x3c, v0\n"
      "s_lshl_b32 s95, s77, 7\n"
      "v_lshl_add_u32 v12, v7, 1, v12\n"
      "s_add_u32 s95, s95, 157696\n"
      "v_rcp_f32 v11, v11\n"
      "v_add_u32 v12, s95, v12\n"
      "v_mul_f32 v10, v10, v11\n"
      "s_cmp_lg_u32 s80, 0\n"
      "s_waitcnt lgkmcnt(0)\n"
      "v_lshl_or_b32 v13, v13, 30, v7\n"
      "s_barrier\n"
      "ds_read_b64 v[16:17], v12\n"
      "v_and_b32 v15, 1, v8\n"
      "v_and_b32 v9, 15, v0\n"
      "v_lshlrev_b32 v29, 4, v9\n"
      "v_lshl_add_u32 v29, v15, 3, v29\n"
      "v_lshlrev_b32 v60, 5, v0\n"
      "v_lshlrev_b32 v11, 1, v9\n"
      "v_and_b32 v28, 0x7fffffff, v28\n"
      "v_lshlrev_b32 v7, 4, v0\n"
      "v_mov_b64 v[24:25], v[92:93]\n"
      "v_lshl_or_b32 v11, v0, 31, v11\n"
      "s_waitcnt lgkmcnt(0)\n"
      "v_mul_f32 v16, v16, v10\n"
      "v_mul_f32 v17, v17, v10\n"
      "v_lshl_add_u32 v11, v15, 13, v11\n"
      "v_add_f32_dpp v16, v16, v16 quad_perm:[1,0,3,2] row_mask:0xf bank_mask:0xf\n"
      "v_add_f32_dpp v17, v17, v17 quad_perm:[1,0,3,2] row_mask:0xf bank_mask:0xf\n"
      "v_and_or_b32 v11, v0, 32, v11\n"
      "v_add_f32_dpp v16, v16, v16 quad_perm:[2,3,0,1] row_mask:0xf bank_mask:0xf\n"
      "v_add_f32_dpp v17, v17, v17 quad_perm:[2,3,0,1] row_mask:0xf bank_mask:0xf\n"
      "v_cvt_pk_bf16_f32 v12, v16, v17\n"
      "v_min_u32 v12, -2, v12\n"
      "buffer_store_dword v12, v13, s[40:43], s32 offen sc1\n"
      "s_cbranch_scc1 L_al_second\n"
      "buffer_load_dwordx2 v[62:63], v29, s[40:43], s48 offen sc1\n"
      "v_bfe_u32 v24, v0, 3, 2\n"
      "s_lshl_b32 s95, s77, 1\n"
      "v_xor_b32 v24, s95, v24\n"
      "v_and_b32 v24, 3, v24\n"
      "v_lshlrev_b32 v24, 5, v24\n"
      "v_and_b32 v25, 0x30, v0\n"
      "v_lshl_add_u32 v24, v25, 8, v24\n"
      "v_and_b32 v25, 12, v0\n"
      "v_lshl_add_u32 v24, v25, 5, v24\n"
      "v_and_b32 v25, 3, v0\n"
      "v_lshl_add_u32 v24, v25, 3, v24\n"
      "v_add_u32 v24, 139264, v24\n"
      "v_xor_b32 v25, 32, v24\n"
      "v_lshlrev_b32 v60, 4, v0\n"
      "L_spin3:\n"
      "v_mov_b32 v0, v0\n"
      "s_getreg_b32 s97, hwreg(HW_REG_IB_STS, 0, 4)\n"
      "s_cmp_gt_u32 s97, 2\n"
      "v_mov_b32 v0, v0\n"
      "s_cbranch_scc1 L_spin3\n"
      "s_waitcnt vmcnt(2)\n"
      "L_s0_chk:\n"
      "v_cmp_eq_u32 vcc, -1, v30\n"
      "s_cbranch_vccnz L_s0_retry\n"
      "L_s0_ok:\n"
      "v_subrev_u32 v10, s6, v30\n"
      "s_nop 1\n"
      "v_max_i32_dpp v10, v10, v10 quad_perm:[1,0,3,2] row_mask:0xf bank_mask:0xf\n"
      "s_add_u32 s94, s6, 20\n"
      "s_nop 0\n"
      "v_max_i32_dpp v10, v10, v10 quad_perm:[2,3,0,1] row_mask:0xf bank_mask:0xf\n"
      "s_nop 0\n"
      "v_readlane_b32 s95, v10, 4\n"
      "s_min_i32 s93, s95, 1000\n"
      "s_add_u32 s93, s93, s94\n"
      "L_al_clk:\n"
      "s_memrealtime s[30:31]\n"
      "s_waitcnt lgkmcnt(0)\n"
      "s_sub_u32 s30, s30, s93\n"
      "L_al_clk_in:\n"
      "s_cmp_lt_i32 s30, 0\n"
      "s_cbranch_scc1 L_al_clk\n"
      "s_barrier\n"
      "buffer_load_dwordx4 v[16:19], v7, s[40:43], s34 offen sc1\n"
      "s_mov_b64 s[98:99], -1\n"
      "ds_read_b64_tr_b16 v[32:33], v24 offset:0\n"
      "ds_read_b64_tr_b16 v[34:35], v24 offset:512\n"
      "v_mov_b32 v0, v0\n"
      "ds_read_b64_tr_b16 v[36:37], v25 offset:0\n"
      "ds_read_b64_tr_b16 v[38:39], v25 offset:512\n"
      "v_mov_b32 v0, v0\n"
      "ds_read_b64_tr_b16 v[40:41], v24 offset:1024\n"
      "ds_read_b64_tr_b16 v[42:43], v24 offset:1536\n"
      "v_mov_b32 v0, v0\n"
      "ds_read_b64_tr_b16 v[44:45], v25 offset:1024\n"
      "ds_read_b64_tr_b16 v[46:47], v25 offset:1536\n"
      "v_mov_b32 v0, v0\n"
      "ds_read_b64_tr_b16 v[64:65], v24 offset:2048\n"
      "ds_read_b64_tr_b16 v[66:67], v24 offset:2560\n"
      "v_mov_b32 v0, v0\n"
      "ds_read_b64_tr_b16 v[68:69], v25 offset:2048\n"
      "ds_read_b64_tr_b16 v[70:71], v25 offset:2560\n"
      "v_mov_b32 v0, v0\n"
      "ds_read_b64_tr_b16 v[72:73], v24 offset:3072\n"
      "ds_read_b64_tr_b16 v[74:75], v24 offset:3584\n"
      "v_mov_b32 v0, v0\n"
      "ds_read_b64_tr_b16 v[76:77], v25 offset:3072\n"
      "ds_read_b64_tr_b16 v[78:79], v25 offset:3584\n"
      "v_mov_b32 v0, v0\n"
      "s_waitcnt vmcnt(1)\n"
      "v_max_f32_dpp v10, v30, v30 quad_perm:[1,0,3,2] row_mask:0xf bank_mask:0xf\n"
      "v_max3_u32 v15, v30, v62, v63\n"
      "s_nop 0\n"
      "v_max_f32_dpp v10, v10, v10 quad_perm:[2,3,0,1] row_mask:0xf bank_mask:0xf\n"
      "v_sub_f32 v13, v30, v10\n"
      "v_exp_f32 v13, v13\n"
      "s_lshl_b32 s95, s77, 4\n"
      "s_nop 0\n"
      "v_add_f32_dpp v10, v13, v13 quad_perm:[1,0,3,2] row_mask:0xf bank_mask:0xf\n"
      "s_add_u32 s95, s95, 163328\n"
      "s_nop 0\n"
      "v_add_f32_dpp v10, v10, v10 quad_perm:[2,3,0,1] row_mask:0xf bank_mask:0xf\n"
      "v_lshrrev_b32 v58, 4, v0\n"
      "v_rcp_f32 v10, v10\n"
      "v_lshl_add_u32 v58, v58, 2, s95\n"
      "v_mul_f32 v13, v13, v10\n"
      "v_mov_b32 v59, 163328\n"
      "s_nop 0\n"
      "v_mov_b32_dpp v10, v13 row_newbcast:0 row_mask:0xf bank_mask:0xf\n"
      "buffer_load_dword v56, v60, s[40:43], s5 offen sc1\n"
      "L_spin4:\n"
      "v_mov_b32 v0, v0\n"
      "s_getreg_b32 s97, hwreg(HW_REG_IB_STS, 0, 4)\n"
      "s_cmp_gt_u32 s97, 1\n"
      "v_mov_b32 v0, v0\n"
      "s_cbranch_scc1 L_spin4\n"
      "s_waitcnt vmcnt(1)\n"
      "s_branch L_al_mm\n"
      "L_stub_a:\n"
      "s_setpc_b64 s[66:67]\n"
      "L_over_a:\n"
      "L_al_second:\n"
      "ds_read_b64_tr_b16 v[32:33], v24 offset:0\n"
      "ds_read_b64_tr_b16 v[34:35], v24 offset:512\n"
      "ds_read_b64_tr_b16 v[36:37], v25 offset:0\n"
      "ds_read_b64_tr_b16 v[38:39], v25 offset:512\n"
      "ds_read_b64_tr_b16 v[40:41], v24 offset:1024\n"
      "ds_read_b64_tr_b16 v[42:43], v24 offset:1536\n"
      "ds_read_b64_tr_b16 v[44:45], v25 offset:1024\n"
      "ds_read_b64_tr_b16 v[46:47], v25 offset:1536\n"
      "ds_read_b64_tr_b16 v[64:65], v24 offset:2048\n"
      "ds_read_b64_tr_b16 v[66:67], v24 offset:2560\n"
      "ds_read_b64_tr_b16 v[68:69], v25 offset:2048\n"
      "ds_read_b64_tr_b16 v[70:71], v25 offset:2560\n"
      "ds_read_b64_tr_b16 v[72:73], v24 offset:3072\n"
      "ds_read_b64_tr_b16 v[74:75], v24 offset:3584\n"
      "ds_read_b64_tr_b16 v[76:77], v25 offset:3072\n"
      "ds_read_b64_tr_b16 v[78:79], v25 offset:3584\n"
      "buffer_load_dwordx4 v[84:87], v60, s[40:43], s5 offen sc1\n"
      "buffer_load_dword v30, v28, s[40:43], s35 offen sc1\n"
      "s_nop 7\n"
      "s_nop 7\n"
      "s_nop 7\n"
      "s_nop 7\n"
      "s_nop 7\n"
      "s_waitcnt vmcnt(4)\n"
      "v_subrev_u32 v10, s6, v82\n"
      "v_cmp_lt_i32 vcc, 10, v10\n"
      "s_cbranch_vccnz L_al_peer\n"
      "L_al_peer_ok:\n"
      "buffer_load_dwordx4 v[16:19], v7, s[40:43], s34 offen sc1\n"
      "v_mov_b64 v[62:63], v[80:81]\n"
      "L_spin5:\n"
      "v_mov_b32 v0, v0\n"
      "s_getreg_b32 s97, hwreg(HW_REG_IB_STS, 0, 4)\n"
      "s_cmp_gt_u32 s97, 1\n"
      "v_mov_b32 v0, v0\n"
      "s_cbranch_scc1 L_spin5\n"
      "s_waitcnt vmcnt(1)\n"
      "v_mov_b32 v56, v84\n"
      "L_al_chk:\n"
      "v_max_f32_dpp v10, v30, v30 quad_perm:[1,0,3,2] row_mask:0xf bank_mask:0xf\n"
      "v_cmp_eq_u32 s[98:99], -1, v56\n"
      "v_subrev_u32 v57, s70, v56\n"
      "v_max3_u32 v15, v30, v62, v63\n"
      "v_max_f32_dpp v10, v10, v10 quad_perm:[2,3,0,1] row_mask:0xf bank_mask:0xf\n"
      "v_max_i32_dpp v57, v57, v57 quad_perm:[1,0,3,2] row_mask:0xf bank_mask:0xf\n"
      "v_sub_f32 v13, v30, v10\n"
      "v_exp_f32 v13, v13\n"
      "v_max_i32_dpp v57, v57, v57 quad_perm:[2,3,0,1] row_mask:0xf bank_mask:0xf\n"
      "s_lshl_b32 s95, s77, 4\n"
      "v_add_f32_dpp v10, v13, v13 quad_perm:[1,0,3,2] row_mask:0xf bank_mask:0xf\n"
      "v_max_i32_dpp v57, v57, v57 row_half_mirror row_mask:0xf bank_mask:0xf\n"
      "s_add_u32 s95, s95, 163328\n"
      "v_add_f32_dpp v10, v10, v10 quad_perm:[2,3,0,1] row_mask:0xf bank_mask:0xf\n"
      "v_max_i32_dpp v57, v57, v57 row_mirror row_mask:0xf bank_mask:0xf\n"
      "v_lshrrev_b32 v58, 4, v0\n"
      "v_rcp_f32 v10, v10\n"
      "v_max_i32_dpp v57, v57, v57 row_bcast:15 row_mask:0xa bank_mask:0xf\n"
      "v_lshl_add_u32 v58, v58, 2, s95\n"
      "v_mul_f32 v13, v13, v10\n"
      "v_max_i32_dpp v57, v57, v57 row_bcast:31 row_mask:0xc bank_mask:0xf\n"
      "v_mov_b32 v59, 163328\n"
      "v_mov_b32_dpp v10, v13 row_newbcast:0 row_mask:0xf bank_mask:0xf\n"
      "ds_write_b32 v58, v57\n"
      "L_spin6:\n"
      "v_mov_b32 v0, v0\n"
      "s_getreg_b32 s97, hwreg(HW_REG_IB_STS, 0, 4)\n"
      "s_cmp_gt_u32 s97, 0\n"
      "v_mov_b32 v0, v0\n"
      "s_cbranch_scc1 L_spin6\n"
      "s_waitcnt vmcnt(0)\n"
      "L_al_mm:\n"
      "s_waitcnt lgkmcnt(0)\n"
      "v_mfma_f32_16x16x32_bf16 v[20:23], v[16:19], v[32:35], 0\n"
      "v_max3_u32 v15, v15, v16, v17\n"
      "v_max3_u32 v15, v15, v18, v19\n"
      "v_mfma_f32_16x16x32_bf16 v[32:35], v[16:19], v[36:39], 0\n"
      "v_cmp_eq_u32 vcc, -1, v15\n"
      "s_nop 0\n"
      "v_mfma_f32_16x16x32_bf16 v[36:39], v[16:19], v[40:43], 0\n"
      "s_nop 1\n"
      "v_mfma_f32_16x16x32_bf16 v[40:43], v[16:19], v[44:47], 0\n"
      "v_mul_f32 v26, v10, v20\n"
      "s_nop 0\n"
      "v_mfma_f32_16x16x32_bf16 v[44:47], v[16:19], v[64:67], 0\n"
      "v_mul_f32 v27, v10, v32\n"
      "s_cbranch_vccnz L_al_retry\n"
      "L_al_ok:\n"
      "v_mfma_f32_16x16x32_bf16 v[64:67], v[16:19], v[68:71], 0\n"
      "v_fmac_f32 v26, v10, v37\n"
      "s_nop 0\n"
      "v_mfma_f32_16x16x32_bf16 v[68:71], v[16:19], v[72:75], 0\n"
      "v_fmac_f32 v27, v10, v41\n"
      "s_nop 0\n"
      "v_mfma_f32_16x16x32_bf16 v[72:75], v[16:19], v[76:79], 0\n"
      "v_fmac_f32 v26, v10, v46\n"
      "s_nop 0\n"
      "v_fmac_f32 v27, v10, v66\n"
      "s_nop 1\n"
      "v_fmac_f32 v26, v10, v71\n"
      "s_nop 1\n"
      "v_fmac_f32 v27, v10, v75\n"
      "s_nop 1\n"
      "v_permlane32_swap_b32 v26, v27\n"
      "v_add_f32 v13, v26, v27\n"
      "v_add_f32 v26, v26, v27\n"
      "s_cmp_gt_u32 s80, s91\n"
      "s_nop 0\n"
      "v_permlane16_swap_b32 v26, v13\n"
      "v_add_f32 v13, v26, v13\n"
      "s_nop 1\n"
      "v_mov_b32_dpp v12, v13 quad_perm:[1,1,3,3] row_mask:0xf bank_mask:0xf\n"
      "v_cvt_pk_bf16_f32 v13, v13, v12\n"
      "v_min_u32 v13, -2, v13\n"
      "buffer_store_dword v13, v11, s[40:43], s49 offen sc1\n"
      "s_cbranch_scc1 L_end\n"
      "s_cmp_lg_u64 s[98:99], 0\n"
      "s_cbranch_scc1 L_ts_retry\n"
      "L_ts_ok:\n"
      "s_barrier\n"
      "ds_read2_b32 v[56:57], v59 offset0:3 offset1:7\n"
      "ds_read2_b32 v[58:59], v59 offset0:11 offset1:15\n"
      "v_and_b32 v7, 15, v0\n"
      "v_lshrrev_b32 v8, 4, v0\n"
      "v_lshlrev_b32 v40, 4, v7\n"
      "v_lshl_add_u32 v10, v8, 10, v40\n"
      "s_cmp_eq_u32 s80, 0\n"
      "s_cselect_b32 s94, -17, 0\n"
      "s_add_u32 s94, s94, s70\n"
      "s_add_u32 s94, s94, 101\n"
      "s_sub_u32 s95, s6, s70\n"
      "s_add_u32 s95, s95, 1000\n"
      "s_waitcnt lgkmcnt(0)\n"
      "v_max_i32 v15, v56, v57\n"
      "v_max3_i32 v15, v15, v58, v59\n"
      "s_nop 0\n"
      "v_readfirstlane_b32 s93, v15\n"
      "s_min_i32 s93, s93, s95\n"
      "s_add_u32 s7, s93, s94\n"
      "s_cmp_eq_u32 s91, 0\n"
      "s_cbranch_scc1 L_gamma\n"
      "s_branch L_over_b\n"
      "L_pend_b:\n"
      ".p2align 12\n"
      "L_stub_b:\n"
      "s_setpc_b64 s[66:67]\n"
      "L_over_b:\n"
      "s_mov_b32 s87, s7\n"
      "s_mov_b32 s92, 0\n"
      "L_be_clk:\n"
      "s_memrealtime s[30:31]\n"
      "s_waitcnt lgkmcnt(0)\n"
      "s_sub_u32 s30, s30, s87\n"
      "L_be_clk_in:\n"
      "s_cmp_lt_i32 s30, -4\n"
      "s_cbranch_scc1 L_be_clk\n"
      "s_cmp_ge_i32 s30, -1\n"
      "s_cbranch_scc1 L_be_clk_go\n"
      "s_nop 3\n"
      "s_cmp_ge_i32 s30, -2\n"
      "s_cbranch_scc1 L_be_clk_go\n"
      "s_nop 3\n"
      "s_cmp_ge_i32 s30, -3\n"
      "s_cbranch_scc1 L_be_clk_go\n"
      "s_nop 7\n"
      "L_be_clk_go:\n"
      "s_xor_b32 s94, s50, 0\n"
      "buffer_load_dwordx4 v[16:19], v10, s[40:43], s94 offen sc1\n"
      "s_xor_b32 s94, s50, 4096\n"
      "buffer_load_dwordx4 v[20:23], v10, s[40:43], s94 offen sc1\n"
      "s_xor_b32 s94, s50, 16384\n"
      "buffer_load_dwordx4 v[24:27], v10, s[40:43], s94 offen sc1\n"
      "s_xor_b32 s94, s50, 20480\n"
      "buffer_load_dwordx4 v[28:31], v10, s[40:43], s94 offen sc1\n"
      "s_xor_b32 s94, s50, 32768\n"
      "buffer_load_dwordx4 v[64:67], v10, s[40:43], s94 offen sc1\n"
      "s_xor_b32 s94, s50, 36864\n"
      "buffer_load_dwordx4 v[68:71], v10, s[40:43], s94 offen sc1\n"
      "s_xor_b32 s94, s50, 49152\n"
      "buffer_load_dwordx4 v[72:75], v10, s[40:43], s94 offen sc1\n"
      "s_xor_b32 s94, s50, 53248\n"
      "buffer_load_dwordx4 v[76:79], v10, s[40:43], s94 offen sc1\n"
      "v_and_b32 v15, 7, v0\n"
      "v_lshlrev_b32 v15, 4, v15\n"
      "v_add_u32 v15, 162752, v15\n"
      "ds_read_b128 v[32:35], v15\n"
      "v_lshlrev_b32 v15, 2, v7\n"
      "v_add_u32 v15, 163008, v15\n"
      "ds_read_b32 v43, v15\n"
      "v_add_u32 v15, 163072, v40\n"
      "ds_read_b128 v[52:55], v15\n"
      "v_and_b32 v9, 1, v8\n"
      "v_lshlrev_b32 v48, 16, v62\n"
      "v_and_b32 v49, 0xffff0000, v62\n"
      "v_lshlrev_b32 v50, 16, v63\n"
      "v_and_b32 v51, 0xffff0000, v63\n"
      "v_mov_b32 v13, s62\n"
      "v_mov_b32 v14, s63\n"
      "v_mov_b32 v12, s62\n"
      "s_nop 0\n"
      "v_mov_b32_dpp v13, v14 quad_perm:[0,1,2,3] row_mask:0xc bank_mask:0xf\n"
      "v_mov_b32_dpp v12, v14 quad_perm:[0,1,2,3] row_mask:0xf bank_mask:0xc\n"
      "v_lshl_add_u32 v13, v9, 17, v13\n"
      "v_lshl_add_u32 v13, v7, 2, v13\n"
      "v_and_b32 v15, 4, v7\n"
      "v_lshl_add_u32 v14, v15, 15, v12\n"
      "v_and_b32 v15, 3, v7\n"
      "v_lshl_add_u32 v14, v15, 4, v14\n"
      "v_lshl_add_u32 v14, v9, 3, v14\n"
      "s_cmp_eq_u32 s88, 0\n"
      "s_cselect_b32 s95, 0, 0x80000000\n"
      "v_or_b32 v14, s95, v14\n"
      "v_and_b32 v15, 32, v0\n"
      "v_lshl_or_b32 v14, v15, 26, v14\n"
      "v_add_u32 v40, 157696, v40\n"
      "v_and_b32 v15, 1, v7\n"
      "v_lshlrev_b32 v41, 9, v15\n"
      "v_add_u32 v41, 158192, v41\n"
      "v_lshlrev_b32 v9, 2, v9\n"
      "v_lshl_add_u32 v9, v15, 3, v9\n"
      "v_lshrrev_b32 v15, 1, v7\n"
      "v_lshl_add_u32 v9, v15, 5, v9\n"
      "s_lshl_b32 s97, s77, 9\n"
      "s_add_u32 s97, s97, 155648\n"
      "v_add_u32 v9, s97, v9\n"
      "s_lshl_b32 s95, s77, 2\n"
      "s_add_u32 s95, s95, 157696\n"
      "v_lshl_add_u32 v12, v0, 4, s95\n"
      "v_mov_b32 v44, 0xbdd2d3e8\n"
      "v_mov_b32 v45, 0x3b000000\n"
      "v_mov_b32 v38, 0x3b003b00\n"
      "v_mov_b32 v39, 0x3b003b00\n"
      "v_mov_b64 v[56:57], 0\n"
      "v_mov_b64 v[60:61], 0\n"
      "v_mov_b64 v[62:63], 0\n"
      "v_mov_b32 v8, 0\n"
      "s_waitcnt lgkmcnt(0)\n"
      "v_add_f32 v52, v52, v53\n"
      "v_add_f32 v54, v54, v55\n"
      "v_add_f32 v46, v52, v54\n"
      "L_be_chk:\n"
      "s_waitcnt vmcnt(7)\n"
      "v_mfma_f32_16x16x32_bf16 v[52:55], v[32:35], v[16:19], v[48:51]\n"
      "v_max3_u32 v15, v16, v16, v17\n"
      "v_max3_u32 v15, v15, v18, v19\n"
      "s_waitcnt vmcnt(6)\n"
      "v_mfma_f32_16x16x32_bf16 v[52:55], v[32:35], v[20:23], v[52:55]\n"
      "v_max3_u32 v15, v15, v20, v21\n"
      "v_max3_u32 v15, v15, v22, v23\n"
      "s_waitcnt vmcnt(5)\n"
      "v_mfma_f32_16x16x32_bf16 v[52:55], v[32:35], v[24:27], v[52:55]\n"
      "v_max3_u32 v15, v15, v24, v25\n"
      "v_max3_u32 v15, v15, v26, v27\n"
      "s_waitcnt vmcnt(4)\n"
      "v_mfma_f32_16x16x32_bf16 v[52:55], v[32:35], v[28:31], v[52:55]\n"
      "v_max3_u32 v15, v15, v28, v29\n"
      "v_max3_u32 v15, v15, v30, v31\n"
      "s_waitcnt vmcnt(3)\n"
      "v_mfma_f32_16x16x32_bf16 v[52:55], v[32:35], v[64:67], v[52:55]\n"
      "v_max3_u32 v15, v15, v64, v65\n"
      "v_max3_u32 v15, v15, v66, v67\n"
      "s_waitcnt vmcnt(2)\n"
      "v_mfma_f32_16x16x32_bf16 v[52:55], v[32:35], v[68:71], v[52:55]\n"
      "v_max3_u32 v15, v15, v68, v69\n"
      "v_max3_u32 v15, v15, v70, v71\n"
      "s_waitcnt vmcnt(1)\n"
      "v_mfma_f32_16x16x32_bf16 v[52:55], v[32:35], v[72:75], v[52:55]\n"
      "v_max3_u32 v15, v15, v72, v73\n"
      "v_max3_u32 v15, v15, v74, v75\n"
      "s_waitcnt vmcnt(0)\n"
      "v_mfma_f32_16x16x32_bf16 v[52:55], v[32:35], v[76:79], v[52:55]\n"
      "v_max3_u32 v15, v15, v76, v77\n"
      "v_max3_u32 v15, v15, v78, v79\n"
      "v_cmp_eq_u32 vcc, -1, v15\n"
      "s_nop 1\n"
      "v_mov_b32 v0, v0\n"
      "s_nop 1\n"
      "v_cvt_pk_bf16_f32 v36, v52, v53\n"
      "v_cvt_pk_bf16_f32 v37, v54, v55\n"
      "s_cbranch_vccnz L_be_retry\n"
      "L_be_ok:\n"
      "ds_write2_b32 v9, v36, v37 offset1:4\n"
      "ds_read_b128 v[16:19], v5\n"
      "ds_read_b128 v[20:23], v5 offset:128\n"
      "v_mov_b32_dpp v38, v36 quad_perm:[0,1,2,3] row_mask:0xc bank_mask:0xf\n"
      "v_mov_b32_dpp v39, v37 quad_perm:[0,1,2,3] row_mask:0xc bank_mask:0xf\n"
      "v_dot2c_f32_bf16 v8, v36, v38\n"
      "v_dot2c_f32_bf16 v8, v37, v39\n"
      "v_min_u32 v36, -2, v36\n"
      "v_min_u32 v37, -2, v37\n"
      "s_nop 0\n"
      "v_add_f32_dpp v8, v8, v8 quad_perm:[1,0,3,2] row_mask:0xf bank_mask:0xf\n"
      "s_nop 1\n"
      "v_add_f32_dpp v8, v8, v8 quad_perm:[2,3,0,1] row_mask:0xf bank_mask:0xf\n"
      "s_nop 1\n"
      "v_add_f32_dpp v8, v8, v8 row_half_mirror row_mask:0xf bank_mask:0xf\n"
      "s_waitcnt lgkmcnt(1)\n"
      "v_smfmac_f32_16x16x64_bf16 v[56:59], v[16:19], v[96:103], v1\n"
      "s_waitcnt lgkmcnt(0)\n"
      "v_smfmac_f32_16x16x64_bf16 v[56:59], v[20:23], v[104:111], v1\n"
      "v_add_f32_dpp v8, v8, v8 row_mirror row_mask:0xf bank_mask:0xf\n"
      "buffer_store_dwordx2 v[36:37], v14, s[40:43], s60 offen sc1\n"
      "s_nop 0\n"
      "v_add_f32_dpp v8, v8, v8 row_bcast:15 row_mask:0xa bank_mask:0xf\n"
      "s_nop 3\n"
      "v_add_f32_dpp v8, v56, v57 quad_perm:[0,1,2,3] row_mask:0x5 bank_mask:0xf\n"
      "ds_write_b32 v12, v8\n"
      "v_mov_b32 v0, v0\n"
      "s_nop 1\n"
      "v_mov_b32 v0, v0\n"
      "s_nop 1\n"
      "v_mov_b32 v0, v0\n"
      "s_nop 1\n"
      "v_mov_b32 v0, v0\n"
      "s_waitcnt lgkmcnt(0)\n"
      "v_mov_b32 v0, v0\n"
      "s_barrier\n"
      "ds_read_b128 v[28:31], v41\n"
      "v_mov_b32 v0, v0\n"
      "ds_read_b128 v[24:27], v40\n"
      "v_mov_b32 v0, v0\n"
      "s_nop 1\n"
      "v_mov_b32 v0, v0\n"
      "s_nop 1\n"
      "v_mov_b32 v0, v0\n"
      "v_mov_b32 v0, v0\n"
      "s_waitcnt lgkmcnt(1)\n"
      "v_pk_add_f32 v[28:29], v[28:29], v[30:31]\n"
      "v_add_f32 v28, v28, v29\n"
      "s_waitcnt lgkmcnt(0)\n"
      "v_pk_add_f32 v[24:25], v[24:25], v[26:27]\n"
      "v_mov_b32_dpp v29, v28 quad_perm:[0,0,0,0] row_mask:0xf bank_mask:0xf\n"
      "v_mul_f32_dpp v30, v28, v45 quad_perm:[1,1,1,1] row_mask:0xf bank_mask:0xf\n"
      "v_fma_f32 v30, -v29, v29, v30\n"
      "v_max_f32 v30, 0, v30\n"
      "v_add_f32 v30, 0x3727c5ac, v30\n"
      "v_rsq_f32 v30, v30\n"
      "v_fma_f32 v24, -v29, v46, v24\n"
      "v_add_f32 v24, v24, v25\n"
      "v_fma_f32 v24, v24, v30, v43\n"
      "v_mul_f32 v25, v24, v24\n"
      "v_fmaak_f32 v25, v25, v44, 0xc0135761\n"
      "v_mul_f32 v25, v25, v24\n"
      "v_exp_f32 v25, v25\n"
      "s_cmp_lg_u32 s90, 0\n"
      "v_add_f32 v25, 1.0, v25\n"
      "v_rcp_f32 v25, v25\n"
      "s_cselect_b32 s93, s80, 0\n"
      "v_mul_f32 v24, v24, v25\n"
      "s_cmp_lg_u32 s93, 0\n"
      "s_nop 0\n"
      "v_mov_b32_dpp v25, v24 quad_perm:[1,1,3,3] row_mask:0xf bank_mask:0xf\n"
      "v_cvt_pk_bf16_f32 v24, v24, v25\n"
      "s_nop 1\n"
      "v_dot2c_f32_bf16_dpp v60, v24, v112 row_newbcast:0 row_mask:0xf bank_mask:0xf\n"
      "v_dot2c_f32_bf16_dpp v61, v24, v113 row_newbcast:0 row_mask:0xf bank_mask:0xf\n"
      "v_dot2c_f32_bf16_dpp v62, v24, v114 row_newbcast:2 row_mask:0xf bank_mask:0xf\n"
      "v_dot2c_f32_bf16_dpp v63, v24, v115 row_newbcast:2 row_mask:0xf bank_mask:0xf\n"
      "v_dot2c_f32_bf16_dpp v60, v24, v116 row_newbcast:4 row_mask:0xf bank_mask:0xf\n"
      "v_dot2c_f32_bf16_dpp v61, v24, v117 row_newbcast:4 row_mask:0xf bank_mask:0xf\n"
      "v_dot2c_f32_bf16_dpp v62, v24, v118 row_newbcast:6 row_mask:0xf bank_mask:0xf\n"
      "v_dot2c_f32_bf16_dpp v63, v24, v119 row_newbcast:6 row_mask:0xf bank_mask:0xf\n"
      "v_dot2c_f32_bf16_dpp v60, v24, v120 row_newbcast:8 row_mask:0xf bank_mask:0xf\n"
      "v_dot2c_f32_bf16_dpp v61, v24, v121 row_newbcast:8 row_mask:0xf bank_mask:0xf\n"
      "v_dot2c_f32_bf16_dpp v62, v24, v122 row_newbcast:10 row_mask:0xf bank_mask:0xf\n"
      "v_dot2c_f32_bf16_dpp v63, v24, v123 row_newbcast:10 row_mask:0xf bank_mask:0xf\n"
      "v_dot2c_f32_bf16_dpp v60, v24, v124 row_newbcast:12 row_mask:0xf bank_mask:0xf\n"
      "v_dot2c_f32_bf16_dpp v61, v24, v125 row_newbcast:12 row_mask:0xf bank_mask:0xf\n"
      "v_dot2c_f32_bf16_dpp v62, v24, v126 row_newbcast:14 row_mask:0xf bank_mask:0xf\n"
      "v_dot2c_f32_bf16_dpp v63, v24, v127 row_newbcast:14 row_mask:0xf bank_mask:0xf\n"
      "s_nop 2\n"
      "v_pk_add_f32 v[60:61], v[60:61], v[62:63]\n"
      "v_cvt_pk_bf16_f32 v60, v60, v61\n"
      "v_min_u32 v60, -2, v60\n"
      "buffer_store_dword v60, v13, s[40:43], s51 offen sc1\n"
      "s_cbranch_scc1 L_end\n"
      "s_branch L_over_g\n"
      "L_stub_g:\n"
      "s_setpc_b64 s[66:67]\n"
      "L_over_g:\n"
      "L_gamma:\n"
      "v_mov_b32 v12, 163392\n"
      "ds_read_b32 v12, v12\n"
      "s_bitcmp1_b32 s77, 0\n"
      "s_cselect_b32 s95, 28, 64\n"
      "v_lshlrev_b32 v7, 4, v0\n"
      "v_cmp_gt_u32 vcc, s95, v0\n"
      "v_mov_b32 v14, 0x80000000\n"
      "s_cmp_eq_u32 s80, 0\n"
      "s_cselect_b32 s87, 3, 0\n"
      "s_add_u32 s87, s87, 90\n"
      "v_cndmask_b32 v8, v14, v7, vcc\n"
      "s_sub_u32 s31, s7, s70\n"
      "s_add_u32 s87, s87, 4\n"
      "s_waitcnt lgkmcnt(0)\n"
      "v_readfirstlane_b32 s30, v12\n"
      "s_cmp_ge_i32 s30, s31\n"
      "s_cselect_b32 s30, s30, s31\n"
      "s_cselect_b32 s92, 0, 4\n"
      "s_add_u32 s7, s30, s70\n"
      "s_add_u32 s87, s87, s7\n"
      "s_sub_u32 s87, s87, s92\n"
      "L_ga_clk:\n"
      "s_memrealtime s[30:31]\n"
      "s_waitcnt lgkmcnt(0)\n"
      "s_sub_u32 s30, s30, s87\n"
      "L_ga_clk_in:\n"
      "s_cmp_lt_i32 s30, -4\n"
      "s_cbranch_scc1 L_ga_clk\n"
      "s_cmp_ge_i32 s30, -1\n"
      "s_cbranch_scc1 L_ga_clk_go\n"
      "s_nop 3\n"
      "s_cmp_ge_i32 s30, -2\n"
      "s_cbranch_scc1 L_ga_clk_go\n"
      "s_nop 3\n"
      "s_cmp_ge_i32 s30, -3\n"
      "s_cbranch_scc1 L_ga_clk_go\n"
      "s_nop 7\n"
      "L_ga_clk_go:\n"
      "buffer_load_dwordx4 v[32:35], v7, s[40:43], s61 offen sc1\n"
      "buffer_load_dwordx4 v[36:39], v7, s[40:43], s61 offen offset:1024 sc1\n"
      "buffer_load_dwordx4 v[40:43], v8, s[40:43], s61 offen offset:2048 sc1\n"
      "s_add_u32 s32, s32, 262144\n"
      "s_add_u32 s33, s33, 131072\n"
      "s_add_u32 s34, s34, 262144\n"
      "s_add_u32 s35, s35, 131072\n"
      "s_lshl_b32 s93, s78, 5\n"
      "s_add_u32 s93, s93, s79\n"
      "s_and_b32 s86, s93, 0xf8\n"
      "s_lshl_b32 s93, s93, 4\n"
      "s_and_b32 s68, s68, 0x80000000\n"
      "s_or_b32 s68, s68, s93\n"
      "s_add_u32 s68, s68, 217088\n"
      "s_add_u32 s68, s68, s84\n"
      "s_and_b32 s93, s77, 2\n"
      "s_lshl_b32 s93, s93, 1\n"
      "s_add_u32 s86, s86, s93\n"
      "s_lshl_b32 s86, s86, 4\n"
      "s_add_u32 s93, s84, 217088\n"
      "s_add_u32 s86, s86, s93\n"
      "s_lshl_b32 s5, s77, 11\n"
      "s_add_u32 s5, s5, s84\n"
      "s_add_u32 s5, s5, 196608\n"
      "s_sub_u32 s48, s48, 1032192\n"
      "s_add_u32 s49, s49, 262144\n"
      "s_xor_b32 s50, s50, 262144\n"
      "s_add_u32 s51, s51, 262144\n"
      "s_add_u32 s60, s60, 262144\n"
      "v_and_b32 v15, 7, v0\n"
      "v_lshlrev_b32 v15, 4, v15\n"
      "v_add_u32 v15, 162752, v15\n"
      "ds_read_b128 v[48:51], v15\n"
      "v_add_u32 v15, 128, v15\n"
      "v_mov_b32 v14, 160000\n"
      "s_and_b32 s97, s77, 1\n"
      "s_cselect_b32 s98, 0, 0xffff\n"
      "s_mov_b32 s99, 0\n"
      "v_cndmask_b32 v15, v14, v15, s[98:99]\n"
      "ds_read_b128 v[52:55], v15\n"
      "s_lshr_b32 s94, s77, 1\n"
      "s_lshl_b32 s94, s94, 8\n"
      "s_lshl_b32 s97, s97, 2\n"
      "s_add_u32 s94, s94, s97\n"
      "s_add_u32 s94, s94, 162240\n"
      "v_and_b32 v9, 7, v0\n"
      "v_lshl_add_u32 v9, v9, 3, s94\n"
      "v_and_b32 v14, 0x30, v0\n"
      "v_lshl_add_u32 v9, v14, 2, v9\n"
      "v_and_b32 v10, 3, v0\n"
      "v_lshlrev_b32 v10, 4, v10\n"
      "v_lshl_add_u32 v10, v14, 2, v10\n"
      "v_and_b32 v14, 4, v0\n"
      "v_lshl_add_u32 v10, v14, 6, v10\n"
      "v_add_u32 v10, 162240, v10\n"
      "v_mov_b32 v16, 0x3f803f80\n"
      "v_mov_b32 v17, 0x3f803f80\n"
      "v_mov_b32 v18, 0x3f803f80\n"
      "v_mov_b32 v19, 0x3f803f80\n"
      "s_lshl_b32 s93, s89, 6\n"
      "s_add_u32 s85, s93, 212992\n"
      "v_and_b32 v12, 7, v0\n"
      "v_lshlrev_b32 v12, 3, v12\n"
      "v_and_b32 v13, 8, v0\n"
      "v_lshl_add_u32 v12, v13, 7, v12\n"
      "v_lshrrev_b32 v14, 4, v0\n"
      "v_lshlrev_b32 v15, 12, v14\n"
      "v_mov_b32 v11, 0x80000000\n"
      "v_add_u32 v12, v15, v12\n"
      "v_lshrrev_b32 v15, 1, v13\n"
      "v_xor_b32 v15, 4, v15\n"
      "v_mov_b32_dpp v12, v11 quad_perm:[0,1,2,3] row_mask:0xc bank_mask:0xf\n"
      "v_lshl_add_u32 v15, v14, 3, v15\n"
      "s_cmp_eq_u32 s77, 0\n"
      "s_cselect_b32 s98, 0x01100101, 0\n"
      "s_mov_b32 s99, 0\n"
      "v_lshlrev_b32 v14, 4, v14\n"
      "v_lshl_add_u32 v14, v13, 10, v14\n"
      "v_cndmask_b32 v13, v11, v15, s[98:99]\n"
      "v_and_b32 v15, 4, v0\n"
      "v_lshl_add_u32 v14, v15, 8, v14\n"
      "v_and_b32 v15, 3, v0\n"
      "v_lshl_add_u32 v14, v15, 2, v14\n"
      "s_lshl_b32 s95, s90, 2\n"
      "s_add_u32 s95, s95, s77\n"
      "s_cmp_eq_u32 s95, 1\n"
      "s_cselect_b32 s95, 0, 0x80000000\n"
      "v_or_b32 v14, s95, v14\n"
      "s_waitcnt lgkmcnt(0)\n"
      "L_spin7:\n"
      "v_mov_b32 v0, v0\n"
      "s_getreg_b32 s97, hwreg(HW_REG_IB_STS, 0, 4)\n"
      "s_cmp_gt_u32 s97, 2\n"
      "v_mov_b32 v0, v0\n"
      "s_cbranch_scc1 L_spin7\n"
      "s_waitcnt vmcnt(2)\n"
      "L_ga_chk:\n"
      "v_mfma_f32_16x16x32_bf16 v[56:59], v[32:35], v[48:51], v[52:55]\n"
      "v_max3_u32 v15, v32, v32, v33\n"
      "v_max3_u32 v15, v15, v34, v35\n"
      "s_waitcnt vmcnt(1)\n"
      "v_mfma_f32_16x16x32_bf16 v[56:59], v[36:39], v[48:51], v[56:59]\n"
      "v_max3_u32 v15, v15, v36, v37\n"
      "v_max3_u32 v15, v15, v38, v39\n"
      "s_waitcnt vmcnt(0)\n"
      "v_mfma_f32_16x16x32_bf16 v[56:59], v[40:43], v[48:51], v[56:59]\n"
      "v_max3_u32 v15, v15, v40, v41\n"
      "v_max3_u32 v15, v15, v42, v43\n"
      "v_cmp_eq_u32 vcc, -1, v15\n"
      "s_nop 1\n"
      "v_mov_b32 v0, v0\n"
      "s_nop 1\n"
      "v_permlane32_swap_b32 v56, v58\n"
      "v_permlane32_swap_b32 v57, v59\n"
      "v_add_f32 v56, v56, v58\n"
      "v_add_f32 v57, v57, v59\n"
      "s_cbranch_vccnz L_ga_retry\n"
      "L_ga_ok:\n"
      "s_nop 0\n"
      "v_permlane16_swap_b32 v56, v57\n"
      "v_add_f32 v56, v56, v57\n"
      "ds_write_b32 v9, v56\n"
      "s_cmp_lg_u32 s80, 0\n"
      "v_mov_b32 v0, v0\n"
      "s_nop 1\n"
      "v_mov_b32 v0, v0\n"
      "s_nop 1\n"
      "v_mov_b32 v0, v0\n"
      "s_nop 1\n"
      "v_mov_b32 v0, v0\n"
      "s_waitcnt lgkmcnt(0)\n"
      "v_mov_b32 v0, v0\n"
      "s_barrier\n"
      "ds_read_b128 v[32:35], v10\n"
      "v_mov_b32 v0, v0\n"
      "s_nop 1\n"
      "v_mov_b32 v0, v0\n"
      "s_nop 1\n"
      "v_mov_b32 v0, v0\n"
      "s_nop 1\n"
      "v_mov_b32 v0, v0\n"
      "s_waitcnt lgkmcnt(0)\n"
      "v_add_f32 v32, v32, v33\n"
      "v_add_f32 v34, v34, v35\n"
      "v_cvt_pk_bf16_f32 v32, v32, v34\n"
      "s_cbranch_scc1 L_final\n"
      "s_nop 0\n"
      "v_mov_b32_dpp v16, v32 quad_perm:[0,0,0,0] row_mask:0xf bank_mask:0x3\n"
      "v_mov_b32_dpp v17, v32 quad_perm:[1,1,1,1] row_mask:0xf bank_mask:0x3\n"
      "v_mov_b32_dpp v18, v32 quad_perm:[2,2,2,2] row_mask:0xf bank_mask:0x3\n"
      "v_mov_b32_dpp v19, v32 quad_perm:[3,3,3,3] row_mask:0xf bank_mask:0x3\n"
      "v_min_u32 v32, -2, v32\n"
      "s_nop 0\n"
      "v_mfma_f32_16x16x32_bf16 v[64:67], v[16:19], v[80:83], 0\n"
      "v_mfma_f32_16x16x32_bf16 v[68:71], v[16:19], v[84:87], 0\n"
      "v_mfma_f32_16x16x32_bf16 v[72:75], v[16:19], v[88:91], 0\n"
      "v_mfma_f32_16x16x32_bf16 v[76:79], v[16:19], v[92:95], 0\n"
      "v_mfma_f32_16x16x32_bf16 v[60:63], v[16:19], v[16:19], 0\n"
      "s_nop 1\n"
      "v_mov_b32 v0, v0\n"
      "s_nop 1\n"
      "v_cvt_pk_bf16_f32 v20, v64, v68\n"
      "v_min_u32 v20, -2, v20\n"
      "v_cvt_pk_bf16_f32 v21, v72, v76\n"
      "v_min_u32 v21, -2, v21\n"
      "buffer_store_dwordx2 v[20:21], v12, s[40:43], s64 offen sc1\n"
      "v_min_u32 v60, -2, v60\n"
      "buffer_store_dword v60, v13, s[40:43], s65 offen sc1\n"
      "buffer_store_dword v32, v14, s[40:43], s85 offen sc1\n"
      "s_mov_b32 s80, 1\n"
      "s_add_u32 s61, s61, 262144\n"
      "s_branch L_delta\n"
      "L_stub_f:\n"
      "s_setpc_b64 s[66:67]\n"
      "L_over_f:\n"
      "L_final:\n"
      "buffer_store_dword v32, v14, s[44:47], s93 offen\n"
      "L_end:\n"
      "s_endpgm\n"
      "L_stub_t:\n"
      "s_setpc_b64 s[66:67]\n"
      "L_over_t:\n"
      "L_ts_retry:\n"
      "s_waitcnt vmcnt(1)\n"
      "s_branch L_ts_look\n"
      "L_ts_loop:\n"
      "s_cmp_eq_u32 s81, 0\n"
      "s_cbranch_scc1 L_ts_ok\n"
      "s_sub_u32 s81, s81, 1\n"
      "s_mov_b64 exec, s[98:99]\n"
      "buffer_load_dword v56, v60, s[40:43], s5 offen sc1\n"
      "s_mov_b64 exec, -1\n"
      "s_waitcnt vmcnt(0)\n"
      "L_ts_look:\n"
      "v_cmp_eq_u32 s[98:99], -1, v56\n"
      "v_subrev_u32 v57, s70, v56\n"
      "s_cmp_lg_u64 s[98:99], 0\n"
      "s_cbranch_scc1 L_ts_loop\n"
      "v_max_i32_dpp v57, v57, v57 quad_perm:[1,0,3,2] row_mask:0xf bank_mask:0xf\n"
      "s_nop 1\n"
      "v_max_i32_dpp v57, v57, v57 quad_perm:[2,3,0,1] row_mask:0xf bank_mask:0xf\n"
      "s_nop 1\n"
      "v_max_i32_dpp v57, v57, v57 row_half_mirror row_mask:0xf bank_mask:0xf\n"
      "s_nop 1\n"
      "v_max_i32_dpp v57, v57, v57 row_mirror row_mask:0xf bank_mask:0xf\n"
      "s_nop 1\n"
      "v_max_i32_dpp v57, v57, v57 row_bcast:15 row_mask:0xa bank_mask:0xf\n"
      "s_nop 1\n"
      "v_max_i32_dpp v57, v57, v57 row_bcast:31 row_mask:0xc bank_mask:0xf\n"
      "ds_write_b32 v58, v57\n"
      "s_waitcnt lgkmcnt(0)\n"
      "s_branch L_ts_ok\n"
      "L_s0_retry:\n"
      "s_cmp_eq_u32 s81, 0\n"
      "s_cbranch_scc1 L_s0_ok\n"
      "s_sub_u32 s81, s81, 1\n"
      "buffer_load_dword v30, v28, s[40:43], s35 offen sc1\n"
      "s_waitcnt vmcnt(0)\n"
      "s_branch L_s0_chk\n"
      "L_g_retry:\n"
      "s_cmp_eq_u32 s81, 0\n"
      "s_cbranch_scc1 L_g_ok\n"
      "s_sub_u32 s81, s81, 1\n"
      "s_lshl_b32 s94, s4, 12\n"
      "s_or_b32 s94, s94, s5\n"
      "v_mov_b32 v56, s94\n"
      "buffer_store_dword v56, v57, s[40:43], s7 offen sc1\n"
      "s_cmp_eq_u32 s6, 0\n"
      "s_cbranch_scc1 L_g_lead\n"
      "s_waitcnt vmcnt(0)\n"
      "s_call_b64 s[66:67], L_lead\n"
      "L_g_lead:\n"
      "buffer_load_dword v59, off, s[40:43], s7 offset:4 sc1\n"
      "s_waitcnt vmcnt(0)\n"
      "s_branch L_g_chk\n"
      "L_stub_i:\n"
      "s_setpc_b64 s[66:67]\n"
      "L_over_i:\n"
      "L_dg_retry:\n"
      "s_cmp_eq_u32 s81, 0\n"
      "s_cbranch_scc1 L_dg_ok\n"
      "s_sub_u32 s81, s81, 1\n"
      "s_add_u32 s7, s7, 42\n"
      "s_add_u32 s87, s87, 42\n"
      "s_add_u32 s87, s87, s92\n"
      "s_mov_b32 s92, 0\n"
      "s_sub_u32 s30, s7, s70\n"
      "v_mov_b32 v15, s30\n"
      "s_mov_b64 exec, 1\n"
      "v_mov_b32 v0, 163392\n"
      "ds_max_i32 v0, v15\n"
      "v_mov_b32 v0, 0\n"
      "s_mov_b64 exec, -1\n"
      "L_dg_again:\n"
      "s_memrealtime s[30:31]\n"
      "s_waitcnt lgkmcnt(0)\n"
      "s_sub_u32 s30, s30, s87\n"
      "L_dg_again_in:\n"
      "s_cmp_lt_i32 s30, -4\n"
      "s_cbranch_scc1 L_dg_again\n"
      "s_cmp_ge_i32 s30, -1\n"
      "s_cbranch_scc1 L_dg_again_go\n"
      "s_nop 3\n"
      "s_cmp_ge_i32 s30, -2\n"
      "s_cbranch_scc1 L_dg_again_go\n"
      "s_nop 3\n"
      "s_cmp_ge_i32 s30, -3\n"
      "s_cbranch_scc1 L_dg_again_go\n"
      "s_nop 7\n"
      "L_dg_again_go:\n"
      "buffer_load_dword v13, v11, s[40:43], s73 offen sc1\n"
      "buffer_load_dwordx4 v[32:35], v10, s[40:43], s72 offen sc1\n"
      "L_spin8:\n"
      "v_mov_b32 v0, v0\n"
      "s_getreg_b32 s97, hwreg(HW_REG_IB_STS, 0, 4)\n"
      "s_cmp_gt_u32 s97, 1\n"
      "v_mov_b32 v0, v0\n"
      "s_cbranch_scc1 L_spin8\n"
      "s_waitcnt vmcnt(1)\n"
      "s_branch L_dg_chk\n"
      "L_stub_r:\n"
      "s_setpc_b64 s[66:67]\n"
      "L_over_r:\n"
      "L_al_retry:\n"
      "s_cmp_eq_u32 s81, 0\n"
      "s_cbranch_scc1 L_al_ok\n"
      "s_sub_u32 s81, s81, 1\n"
      "buffer_load_dword v30, v28, s[40:43], s35 offen sc1\n"
      "buffer_load_dwordx2 v[62:63], v29, s[40:43], s48 offen sc1\n"
      "buffer_load_dwordx4 v[16:19], v7, s[40:43], s34 offen sc1\n"
      "ds_read_b64_tr_b16 v[32:33], v24 offset:0\n"
      "ds_read_b64_tr_b16 v[34:35], v24 offset:512\n"
      "v_mov_b32 v0, v0\n"
      "ds_read_b64_tr_b16 v[36:37], v25 offset:0\n"
      "ds_read_b64_tr_b16 v[38:39], v25 offset:512\n"
      "v_mov_b32 v0, v0\n"
      "ds_read_b64_tr_b16 v[40:41], v24 offset:1024\n"
      "ds_read_b64_tr_b16 v[42:43], v24 offset:1536\n"
      "v_mov_b32 v0, v0\n"
      "ds_read_b64_tr_b16 v[44:45], v25 offset:1024\n"
      "ds_read_b64_tr_b16 v[46:47], v25 offset:1536\n"
      "v_mov_b32 v0, v0\n"
      "ds_read_b64_tr_b16 v[64:65], v24 offset:2048\n"
      "ds_read_b64_tr_b16 v[66:67], v24 offset:2560\n"
      "v_mov_b32 v0, v0\n"
      "ds_read_b64_tr_b16 v[68:69], v25 offset:2048\n"
      "ds_read_b64_tr_b16 v[70:71], v25 offset:2560\n"
      "v_mov_b32 v0, v0\n"
      "ds_read_b64_tr_b16 v[72:73], v24 offset:3072\n"
      "ds_read_b64_tr_b16 v[74:75], v24 offset:3584\n"
      "v_mov_b32 v0, v0\n"
      "ds_read_b64_tr_b16 v[76:77], v25 offset:3072\n"
      "ds_read_b64_tr_b16 v[78:79], v25 offset:3584\n"
      "v_mov_b32 v0, v0\n"
      "s_waitcnt vmcnt(0)\n"
      "s_branch L_al_chk\n"
      "L_stub_p:\n"
      "s_setpc_b64 s[66:67]\n"
      "L_over_p:\n"
      "L_al_peer:\n"
      "v_max_i32_dpp v10, v10, v10 quad_perm:[1,0,3,2] row_mask:0xf bank_mask:0xf\n"
      "s_nop 1\n"
      "v_max_i32_dpp v10, v10, v10 quad_perm:[2,3,0,1] row_mask:0xf bank_mask:0xf\n"
      "s_nop 0\n"
      "v_readfirstlane_b32 s95, v10\n"
      "s_min_i32 s93, s95, 100\n"
      "s_add_u32 s93, s93, s6\n"
      "s_add_u32 s93, s93, 4\n"
      "s_cmp_lt_i32 s95, 20\n"
      "s_cbranch_scc1 L_al_clk1\n"
      "s_sub_u32 s94, s93, 14\n"
      "L_al_clk2:\n"
      "s_memrealtime s[30:31]\n"
      "s_waitcnt lgkmcnt(0)\n"
      "s_sub_u32 s30, s30, s94\n"
      "L_al_clk2_in:\n"
      "s_cmp_lt_i32 s30, 0\n"
      "s_cbranch_scc1 L_al_clk2\n"
      "buffer_load_dword v30, v28, s[40:43], s35 offen sc1\n"
      "L_al_clk1:\n"
      "s_memrealtime s[30:31]\n"
      "s_waitcnt lgkmcnt(0)\n"
      "s_sub_u32 s30, s30, s93\n"
      "L_al_clk1_in:\n"
      "s_cmp_lt_i32 s30, 0\n"
      "s_cbranch_scc1 L_al_clk1\n"
      "s_branch L_al_peer_ok\n"
      "L_stub_e:\n"
      "s_setpc_b64 s[66:67]\n"
      "L_over_e:\n"
      "L_be_retry:\n"
      "s_cmp_eq_u32 s81, 0\n"
      "s_cbranch_scc1 L_be_ok\n"
      "s_sub_u32 s81, s81, 1\n"
      "s_add_u32 s7, s7, 42\n"
      "s_add_u32 s87, s87, 42\n"
      "s_add_u32 s87, s87, s92\n"
      "s_mov_b32 s92, 0\n"
      "s_sub_u32 s30, s7, s70\n"
      "v_mov_b32 v15, s30\n"
      "s_mov_b64 exec, 1\n"
      "v_mov_b32 v0, 163392\n"
      "ds_max_i32 v0, v15\n"
      "v_mov_b32 v0, 0\n"
      "s_mov_b64 exec, -1\n"
      "L_be_again:\n"
      "s_memrealtime s[30:31]\n"
      "s_waitcnt lgkmcnt(0)\n"
      "s_sub_u32 s30, s30, s87\n"
      "L_be_again_in:\n"
      "s_cmp_lt_i32 s30, -4\n"
      "s_cbranch_scc1 L_be_again\n"
      "s_cmp_ge_i32 s30, -1\n"
      "s_cbranch_scc1 L_be_again_go\n"
      "s_nop 3\n"
      "s_cmp_ge_i32 s30, -2\n"
      "s_cbranch_scc1 L_be_again_go\n"
      "s_nop 3\n"
      "s_cmp_ge_i32 s30, -3\n"
      "s_cbranch_scc1 L_be_again_go\n"
      "s_nop 7\n"
      "L_be_again_go:\n"
      "s_xor_b32 s94, s50, 0\n"
      "buffer_load_dwordx4 v[16:19], v10, s[40:43], s94 offen sc1\n"
      "s_xor_b32 s94, s50, 4096\n"
      "buffer_load_dwordx4 v[20:23], v10, s[40:43], s94 offen sc1\n"
      "s_xor_b32 s94, s50, 16384\n"
      "buffer_load_dwordx4 v[24:27], v10, s[40:43], s94 offen sc1\n"
      "s_xor_b32 s94, s50, 20480\n"
      "buffer_load_dwordx4 v[28:31], v10, s[40:43], s94 offen sc1\n"
      "s_xor_b32 s94, s50, 32768\n"
      "buffer_load_dwordx4 v[64:67], v10, s[40:43], s94 offen sc1\n"
      "s_xor_b32 s94, s50, 36864\n"
      "buffer_load_dwordx4 v[68:71], v10, s[40:43], s94 offen sc1\n"
      "s_xor_b32 s94, s50, 49152\n"
      "buffer_load_dwordx4 v[72:75], v10, s[40:43], s94 offen sc1\n"
      "s_xor_b32 s94, s50, 53248\n"
      "buffer_load_dwordx4 v[76:79], v10, s[40:43], s94 offen sc1\n"
      "L_spin9:\n"
      "v_mov_b32 v0, v0\n"
      "s_getreg_b32 s97, hwreg(HW_REG_IB_STS, 0, 4)\n"
      "s_cmp_gt_u32 s97, 7\n"
      "v_mov_b32 v0, v0\n"
      "s_cbranch_scc1 L_spin9\n"
      "s_waitcnt vmcnt(7)\n"
      "s_branch L_be_chk\n"
      "L_stub_h:\n"
      "s_setpc_b64 s[66:67]\n"
      "L_over_h:\n"
      "L_ga_retry:\n"
      "s_cmp_eq_u32 s81, 0\n"
      "s_cbranch_scc1 L_ga_ok\n"
      "s_sub_u32 s81, s81, 1\n"
      "s_add_u32 s7, s7, 42\n"
      "s_add_u32 s87, s87, 42\n"
      "s_add_u32 s87, s87, s92\n"
      "s_mov_b32 s92, 0\n"
      "s_sub_u32 s30, s7, s70\n"
      "v_mov_b32 v15, s30\n"
      "s_mov_b64 exec, 1\n"
      "v_mov_b32 v0, 163392\n"
      "ds_max_i32 v0, v15\n"
      "v_mov_b32 v0, 0\n"
      "s_mov_b64 exec, -1\n"
      "L_ga_again:\n"
      "s_memrealtime s[30:31]\n"
      "s_waitcnt lgkmcnt(0)\n"
      "s_sub_u32 s30, s30, s87\n"
      "L_ga_again_in:\n"
      "s_cmp_lt_i32 s30, -4\n"
      "s_cbranch_scc1 L_ga_again\n"
      "s_cmp_ge_i32 s30, -1\n"
      "s_cbranch_scc1 L_ga_again_go\n"
      "s_nop 3\n"
      "s_cmp_ge_i32 s30, -2\n"
      "s_cbranch_scc1 L_ga_again_go\n"
      "s_nop 3\n"
      "s_cmp_ge_i32 s30, -3\n"
      "s_cbranch_scc1 L_ga_again_go\n"
      "s_nop 7\n"
      "L_ga_again_go:\n"
      "buffer_load_dwordx4 v[32:35], v7, s[40:43], s61 offen sc1\n"
      "buffer_load_dwordx4 v[36:39], v7, s[40:43], s61 offen offset:1024 sc1\n"
      "buffer_load_dwordx4 v[40:43], v8, s[40:43], s61 offen offset:2048 sc1\n"
      "L_spin10:\n"
      "v_mov_b32 v0, v0\n"
      "s_getreg_b32 s97, hwreg(HW_REG_IB_STS, 0, 4)\n"
      "s_cmp_gt_u32 s97, 2\n"
      "v_mov_b32 v0, v0\n"
      "s_cbranch_scc1 L_spin10\n"
      "s_waitcnt vmcnt(2)\n"
      "s_branch L_ga_chk\n"
      "L_last:\n"
      "s_endpgm\n"
      :
      : "s"(__builtin_amdgcn_kernarg_segment_ptr()), "s"(blockIdx.x), "v"(threadIdx.x),
        "s"((unsigned)(uintptr_t)(__attribute__((address_space(3))) char*)lds)
      : "v1", "v2", "v3", "v4", "v5", "v6", "v7", "v8", "v9", "v10", "v11", "v12", "v13", "v14", "v15", "v16",
        "v17", "v18", "v19", "v20", "v21", "v22", "v23", "v24", "v25", "v26", "v27", "v28", "v29", "v30", "v31", "v32",
        "v33", "v34", "v35", "v36", "v37", "v38", "v39", "v40", "v41", "v42", "v43", "v44", "v45", "v46", "v47", "v48",
        "v49", "v50", "v51", "v52", "v53", "v54", "v55", "v56", "v57", "v58", "v59", "v60", "v61", "v62", "v63", "v64",
        "v65", "v66", "v67", "v68", "v69", "v70", "v71", "v72", "v73", "v74", "v75", "v76", "v77", "v78", "v79", "v80",
        "v81", "v82", "v83", "v84", "v85", "v86", "v87", "v88", "v89", "v90", "v91", "v92", "v93", "v94", "v95", "v96",
        "v97", "v98", "v99", "v100", "v101", "v102", "v103", "v104", "v105", "v106", "v107", "v108", "v109", "v110", "v111", "v112",
        "v113", "v114", "v115", "v116", "v117", "v118", "v119", "v120", "v121", "v122", "v123", "v124", "v125", "v126", "v127", "v128",
        "v129", "v130", "v131", "v132", "v133", "v134", "v135", "v136", "v137", "v138", "v139", "v140", "v141", "v142", "v143", "v144",
        "v145", "v146", "v147", "v148", "v149", "v150", "v151", "v152", "v153", "v154", "v155", "v156", "v157", "v158", "v159", "v160",
        "v161", "v162", "v163", "v164", "v165", "v166", "v167", "v168", "v169", "v170", "v171", "v172", "v173", "v174", "v175", "v176",
        "v177", "v178", "v179", "v180", "v181", "v182", "v183", "v184", "v185", "v186", "v187", "v188", "v189", "v190", "v191", "v192",
        "v193", "v194", "v195", "v196", "v197", "v198", "v199", "v200", "v201", "v202", "v203", "v204", "v205", "v206", "v207", "v208",
        "v209", "v210", "v211", "v212", "v213", "v214", "v215", "v216", "v217", "v218", "v219", "v220", "v221", "v222", "v223", "v224",
        "v225", "v226", "v227", "v228", "v229", "v230", "v231", "v232", "v233", "v234", "v235", "v236", "v237", "v238", "v239", "v240",
        "v241", "v242", "v243", "v244", "v245", "v246", "v247", "v248", "v249", "v250", "v251", "v252", "v253", "v254", "v255", "a0",
        "a1", "a2", "a3", "a4", "a5", "a6", "a7", "a8", "a9", "a10", "a11", "a12", "a13", "a14", "a15", "a16",
        "a17", "a18", "a19", "a20", "a21", "a22", "a23", "a24", "a25", "a26", "a27", "a28", "a29", "a30", "a31", "a32",
        "a33", "a34", "a35", "a36", "a37", "a38", "a39", "a40", "a41", "a42", "a43", "a44", "a45", "a46", "a47", "a48",
        "a49", "a50", "a51", "a52", "a53", "a54", "a55", "a56", "a57", "a58", "a59", "a60", "a61", "a62", "a63", "a64",
        "a65", "a66", "a67", "a68", "a69", "a70", "a71", "a72", "a73", "a74", "a75", "a76", "a77", "a78", "a79", "a80",
        "a81", "a82", "a83", "a84", "a85", "a86", "a87", "a88", "a89", "a90", "a91", "a92", "a93", "a94", "a95", "a96",
        "a97", "a98", "a99", "a100", "a101", "a102", "a103", "a104", "a105", "a106", "a107", "a108", "a109", "a110", "a111", "a112",
        "a113", "a114", "a115", "a116", "a117", "a118", "a119", "a120", "a121", "a122", "a123", "a124", "a125", "a126", "a127", "a128",
        "a129", "a130", "a131", "a132", "a133", "a134", "a135", "a136", "a137", "a138", "a139", "a140", "a141", "a142", "a143", "a144",
        "a145", "a146", "a147", "a148", "a149", "a150", "a151", "a152", "a153", "a154", "a155", "a156", "a157", "a158", "a159", "a160",
        "a161", "a162", "a163", "a164", "a165", "a166", "a167", "a168", "a169", "a170", "a171", "a172", "a173", "a174", "a175", "a176",
        "a177", "a178", "a179", "a180", "a181", "a182", "a183", "a184", "a185", "a186", "a187", "a188", "a189", "a190", "a191", "a192",
        "a193", "a194", "a195", "a196", "a197", "a198", "a199", "a200", "a201", "a202", "a203", "a204", "a205", "a206", "a207", "a208",
        "a209", "a210", "a211", "a212", "a213", "a214", "a215", "a216", "a217", "a218", "a219", "a220", "a221", "a222", "a223", "a224",
        "a225", "a226", "a227", "a228", "a229", "a230", "a231", "a232", "a233", "a234", "a235", "a236", "a237", "a238", "a239", "a240",
        "a241", "a242", "a243", "a244", "a245", "a246", "a247", "a248", "a249", "a250", "a251", "a252", "a253", "a254", "a255", "s4",
        "s5", "s6", "s7", "s8", "s9", "s10", "s11", "s12", "s13", "s14", "s15", "s16", "s17", "s18", "s19", "s20",
        "s21", "s22", "s23", "s24", "s25", "s26", "s27", "s28", "s29", "s30", "s31", "s33", "s34", "s35", "s36", "s37",
        "s38", "s39", "s40", "s41", "s42", "s43", "s44", "s45", "s46", "s47", "s48", "s49", "s50", "s51", "s52", "s53",
        "s54", "s55", "s56", "s57", "s58", "s59", "s60", "s61", "s62", "s63", "s64", "s65", "s66", "s67", "s68", "s69",
        "s70", "s71", "s72", "s73", "s74", "s75", "s76", "s77", "s78", "s79", "s80", "s81", "s82", "s83", "s84", "s85",
        "s86", "s87", "s88", "s89", "s90", "s91", "s92", "s93", "s94", "s95", "s96", "s97", "s98", "s99", "vcc", "scc",
        "memory");
}

extern "C" void kernel_launch(void* const* d_in, const int* in_sizes, int n_in, void* d_out, int out_size, void* d_ws,
                              size_t ws_size, hipStream_t stream) {
  char* ws = (char*)(((uintptr_t)d_ws + 0x1fffff) & ~(uintptr_t)0x1fffff) + ((size_t)0 << 20);
  decode_layers_kernel<<<256, 256, 0, stream>>>(d_in[0], d_in[1], d_in[2], d_in[3], d_in[4], d_in[6], d_in[7], d_in[8],
                                                d_in[9], d_out, ws);
}
```
